```python
import torch

def _softmax(x, axis=-1):
    return torch.softmax(x, dim=axis)

def _gelu(x):
    return torch.nn.functional.gelu(x, approximate='tanh')

def _silu(x):
    return torch.nn.functional.silu(x)

def _sigmoid(x):
    return torch.sigmoid(x)

def _layernorm(x, axis=-1, eps=1e-05):
    m = x.mean(dim=axis, keepdim=True)
    v = x.var(dim=axis, keepdim=True, unbiased=False)
    return (x - m) / torch.sqrt(v + eps)

def _scatter_add(tgt, idx, src, axis=0):
    return tgt.clone().index_add_(axis, idx, src)

def _segment_sum(src, seg, n):
    out = torch.zeros((n,) + src.shape[1:], dtype=src.dtype)
    return out.index_add_(0, seg, src)

def _pad(x, pw):
    flat = tuple((v for pair in reversed(pw) for v in pair))
    return torch.nn.functional.pad(x, flat)
kernel_name = 'standalone__dilated_atrous__vision'

def setup_inputs():

    _w = lambda a: (lambda t: t.to(torch.bfloat16) if t.is_floating_point() else t)(torch.as_tensor(a))
    H = W = 384
    C = 96
    k = 3
    return {'x': _w((lambda *s: torch.randn(*s, dtype=torch.bfloat16) * 0.05)(1, H, W, C)), 'w': _w((lambda *s: torch.randn(*s, dtype=torch.bfloat16) * 0.05)(k, k, C, C) * (1.0 / (k * C ** 0.5)))}

def reference(x, w):
    B, H, W, C = x.shape
    k = 3
    rate = 2
    pad = (k - 1) * rate // 2
    xp = _pad(x, ((0, 0), (pad, pad), (pad, pad), (0, 0)))
    out = torch.zeros_like(x)
    for i in range(k):
        for j in range(k):
            tap = xp[:, i * rate:i * rate + H, j * rate:j * rate + W, :].clone()
            out = out + torch.einsum('bhwc,co->bhwo', tap, w[i, j])
    return out
```

```cpp
#include <hip/hip_runtime.h>
#include <hip/hip_bf16.h>

__global__ void naive_kernel(const __hip_bfloat16* __restrict__ x, const __hip_bfloat16* __restrict__ w, __hip_bfloat16* __restrict__ out) {
    const int H = 384, W = 384, C = 96;
    int idx = blockIdx.x * blockDim.x + threadIdx.x;
    if (idx >= H * W * (C / 4)) return;
    int o4 = idx % (C / 4);
    int pix = idx / (C / 4);
    int h = pix / W, wc = pix % W;
    float acc[4] = {0, 0, 0, 0};
    for (int i = 0; i < 3; ++i) {
        int hh = h + 2 * (i - 1);
        if (hh < 0 || hh >= H) continue;
        for (int j = 0; j < 3; ++j) {
            int ww = wc + 2 * (j - 1);
            if (ww < 0 || ww >= W) continue;
            const __hip_bfloat16* xp = x + ((size_t)hh * W + ww) * C;
            const __hip_bfloat16* wp = w + (size_t)(i * 3 + j) * C * C + o4 * 4;
            for (int c = 0; c < C; ++c) {
                float xv = __bfloat162float(xp[c]);
                for (int e = 0; e < 4; ++e) acc[e] += xv * __bfloat162float(wp[c * C + e]);
            }
        }
    }
    for (int e = 0; e < 4; ++e) out[(size_t)pix * C + o4 * 4 + e] = __float2bfloat16(acc[e]);
}

extern "C" void kernel_launch(void* const* d_in, const int* in_sizes, int n_in,
                              void* d_out, int out_size, void* d_ws, size_t ws_size,
                              hipStream_t stream) {
    const __hip_bfloat16* x = (const __hip_bfloat16*)d_in[0];
    const __hip_bfloat16* w = (const __hip_bfloat16*)d_in[1];
    __hip_bfloat16* out = (__hip_bfloat16*)d_out;
    int n = 384 * 384 * 24;
    naive_kernel<<<(n + 255) / 256, 256, 0, stream>>>(x, w, out);
}
```

```cpp
#include <hip/hip_runtime.h>
#include <hip/hip_bf16.h>

__global__ __launch_bounds__(256, 1) void conv_kernel(const __hip_bfloat16* __restrict__ x, const __hip_bfloat16* __restrict__ w,
                                                      __hip_bfloat16* __restrict__ out) {
  __shared__ __attribute__((aligned(1024))) char smem[163840];
  const unsigned ldsb = (unsigned)(size_t)smem;
  asm volatile(
    "s_load_dwordx4 s[8:11], %0, 0x0\n\t"
    "s_load_dwordx2 s[12:13], %0, 0x10\n\t"
    "s_getpc_b64 s[16:17]\n\t"
    "L_pc_%=:\n\t"
    "v_lshlrev_b32 v24, 7, %2\n\t"
    "v_min_u32 v24, L_end_%=-L_pc_%=-4, v24\n\t"
    "global_load_dword v187, v24, s[16:17]\n\t"
    "v_readfirstlane_b32 s58, %2\n\t"
    "v_and_b32 v25, 63, %2\n\t"
    "s_lshr_b32 s58, s58, 6\n\t"
    "s_bfe_u32 s61, %1, 0x20003\n\t"
    "s_mul_i32 s64, s58, 5120\n\t"
    "s_add_u32 s53, s64, %3\n\t"
    "v_lshl_add_u32 v1, v25, 4, s64\n\t"
    "s_mul_i32 s65, s58, 320\n\t"
    "v_add_u32 v26, s65, v25\n\t"
    "s_mul_i32 s68, s61, 18432\n\t"
    "s_sub_u32 s68, s68, 384\n\t"
    "s_cmp_eq_u32 s61, 0\n\t"
    "s_cselect_b32 s66, 24, 0\n\t"
    "s_cmp_eq_u32 s61, 3\n\t"
    "s_cselect_b32 s67, 24, 0\n\t"
    "s_sub_u32 s67, 1200, s67\n\t"
    "s_sub_u32 s67, s67, s66\n\t"
    "v_mov_b32 v30, 1073741824\n\t"
    "s_movk_i32 s64, 1366\n\t"
    "s_and_b32 s62, %1, 7\n\t"
    "s_mul_i32 s62, s62, 48\n\t"
    "s_lshr_b32 s65, %1, 6\n\t"
    "s_and_b32 s63, s65, 1\n\t"
    "s_mul_i32 s65, s65, 12\n\t"
    "s_add_u32 s62, s62, s65\n\t"
    "s_bfe_u32 s65, %1, 0x10005\n\t"
    "s_add_u32 s62, s62, s65\n\t"
    "v_mul_u32_u24 v28, s64, v26\n\t"
    "v_lshrrev_b32 v28, 11, v28\n\t"
    "v_and_b32 v28, 32, v28\n\t"
    "v_lshlrev_b32 v29, 4, v26\n\t"
    "v_xor_b32 v29, v29, v28\n\t"
    "v_add_u32 v29, s68, v29\n\t"
    "v_subrev_u32 v28, s66, v26\n\t"
    "v_cmp_gt_u32 vcc, s67, v28\n\t"
    "s_nop 1\n\t"
    "v_cndmask_b32 v2, v30, v29, vcc\n\t"
    "v_add_u32 v27, 64, v26\n\t"
    "v_mul_u32_u24 v28, s64, v27\n\t"
    "v_lshrrev_b32 v28, 11, v28\n\t"
    "v_and_b32 v28, 32, v28\n\t"
    "v_lshlrev_b32 v29, 4, v27\n\t"
    "v_xor_b32 v29, v29, v28\n\t"
    "v_add_u32 v29, s68, v29\n\t"
    "v_subrev_u32 v28, s66, v27\n\t"
    "v_cmp_gt_u32 vcc, s67, v28\n\t"
    "s_nop 1\n\t"
    "v_cndmask_b32 v3, v30, v29, vcc\n\t"
    "v_add_u32 v27, 128, v26\n\t"
    "v_mul_u32_u24 v28, s64, v27\n\t"
    "v_lshrrev_b32 v28, 11, v28\n\t"
    "v_and_b32 v28, 32, v28\n\t"
    "v_lshlrev_b32 v29, 4, v27\n\t"
    "v_xor_b32 v29, v29, v28\n\t"
    "v_add_u32 v29, s68, v29\n\t"
    "v_subrev_u32 v28, s66, v27\n\t"
    "v_cmp_gt_u32 vcc, s67, v28\n\t"
    "s_nop 1\n\t"
    "v_cndmask_b32 v4, v30, v29, vcc\n\t"
    "v_add_u32 v27, 192, v26\n\t"
    "v_mul_u32_u24 v28, s64, v27\n\t"
    "v_lshrrev_b32 v28, 11, v28\n\t"
    "v_and_b32 v28, 32, v28\n\t"
    "v_lshlrev_b32 v29, 4, v27\n\t"
    "v_xor_b32 v29, v29, v28\n\t"
    "v_add_u32 v29, s68, v29\n\t"
    "v_subrev_u32 v28, s66, v27\n\t"
    "v_cmp_gt_u32 vcc, s67, v28\n\t"
    "s_nop 1\n\t"
    "v_cndmask_b32 v5, v30, v29, vcc\n\t"
    "v_add_u32 v27, 256, v26\n\t"
    "v_mul_u32_u24 v28, s64, v27\n\t"
    "v_lshrrev_b32 v28, 11, v28\n\t"
    "v_and_b32 v28, 32, v28\n\t"
    "v_lshlrev_b32 v29, 4, v27\n\t"
    "v_xor_b32 v29, v29, v28\n\t"
    "v_add_u32 v29, s68, v29\n\t"
    "v_subrev_u32 v28, s66, v27\n\t"
    "v_cmp_gt_u32 vcc, s67, v28\n\t"
    "s_nop 1\n\t"
    "v_cndmask_b32 v6, v30, v29, vcc\n\t"
    "s_mov_b32 s56, 4294819840\n\t"
    "s_cmp_eq_u32 s63, 0\n\t"
    "s_cselect_b32 s65, 0, 10\n\t"
    "s_cselect_b32 s56, 147456, s56\n\t"
    "s_cselect_b32 s50, 0, 110592\n\t"
    "s_cselect_b32 s52, 110592, 0\n\t"
    "s_mov_b32 s51, 55296\n\t"
    "s_add_u32 s65, s65, s62\n\t"
    "s_mul_i32 s37, s65, 73728\n\t"
    "s_sub_u32 s64, s62, 2\n\t"
    "s_mul_i32 s64, s64, 73728\n\t"
    "s_cmp_ge_u32 s62, 2\n\t"
    "s_cselect_b32 s64, s64, 1073741824\n\t"
    "s_add_u32 s65, s62, 12\n\t"
    "s_mul_i32 s59, s65, 73728\n\t"
    "s_cmp_lt_u32 s65, 384\n\t"
    "s_cselect_b32 s65, s59, 1073741824\n\t"
    "s_cmp_eq_u32 s63, 0\n\t"
    "s_cselect_b32 s36, s64, s65\n\t"
    "s_cselect_b32 s43, s65, s64\n\t"
    "s_add_u32 s38, s37, s56\n\t"
    "s_add_u32 s39, s38, s56\n\t"
    "s_add_u32 s40, s39, s56\n\t"
    "s_add_u32 s41, s40, s56\n\t"
    "s_add_u32 s42, s41, s56\n\t"
    "s_cmp_eq_u32 s58, 3\n\t"
    "s_cselect_b64 s[54:55], 0, -1\n\t"
    "s_mov_b32 s22, 28311552\n\t"
    "s_mov_b32 s23, 0x20000\n\t"
    "s_mov_b32 s26, 165888\n\t"
    "s_mov_b32 s27, 0x20000\n\t"
    "s_waitcnt lgkmcnt(0)\n\t"
    "s_mov_b32 s20, s8\n\t"
    "s_and_b32 s21, s9, 0xffff\n\t"
    "s_mov_b32 s24, s10\n\t"
    "s_and_b32 s25, s11, 0xffff\n\t"
    "s_add_u32 m0, s53, 0\n\t"
    "s_nop 0\n\t"
    "buffer_load_dwordx4 v2, s[20:23], s36 offen lds\n\t"
    "s_add_u32 m0, s53, 1024\n\t"
    "s_nop 0\n\t"
    "buffer_load_dwordx4 v3, s[20:23], s36 offen lds\n\t"
    "s_add_u32 m0, s53, 2048\n\t"
    "s_nop 0\n\t"
    "buffer_load_dwordx4 v4, s[20:23], s36 offen lds\n\t"
    "s_add_u32 m0, s53, 3072\n\t"
    "s_nop 0\n\t"
    "buffer_load_dwordx4 v5, s[20:23], s36 offen lds\n\t"
    "s_add_u32 m0, s53, 4096\n\t"
    "s_nop 0\n\t"
    "buffer_load_dwordx4 v6, s[20:23], s36 offen lds\n\t"
    "s_add_u32 m0, s53, 20480\n\t"
    "s_add_u32 s76, s50, 0\n\t"
    "s_nop 0\n\t"
    "buffer_load_dwordx4 v1, s[24:27], s76 offen lds\n\t"
    "s_add_u32 m0, s53, 21504\n\t"
    "s_add_u32 s76, s50, 1024\n\t"
    "s_nop 0\n\t"
    "buffer_load_dwordx4 v1, s[24:27], s76 offen lds\n\t"
    "s_add_u32 m0, s53, 22528\n\t"
    "s_add_u32 s76, s50, 2048\n\t"
    "s_nop 0\n\t"
    "buffer_load_dwordx4 v1, s[24:27], s76 offen lds\n\t"
    "s_add_u32 m0, s53, 23552\n\t"
    "s_add_u32 s76, s50, 3072\n\t"
    "s_nop 0\n\t"
    "buffer_load_dwordx4 v1, s[24:27], s76 offen lds\n\t"
    "s_add_u32 m0, s53, 24576\n\t"
    "s_add_u32 s76, s50, 4096\n\t"
    "s_nop 0\n\t"
    "buffer_load_dwordx4 v1, s[24:27], s76 offen lds\n\t"
    "s_add_u32 m0, s53, 40960\n\t"
    "s_nop 0\n\t"
    "buffer_load_dwordx4 v2, s[20:23], s37 offen lds\n\t"
    "s_add_u32 m0, s53, 41984\n\t"
    "s_nop 0\n\t"
    "buffer_load_dwordx4 v3, s[20:23], s37 offen lds\n\t"
    "s_add_u32 m0, s53, 43008\n\t"
    "s_nop 0\n\t"
    "buffer_load_dwordx4 v4, s[20:23], s37 offen lds\n\t"
    "s_add_u32 m0, s53, 44032\n\t"
    "s_nop 0\n\t"
    "buffer_load_dwordx4 v5, s[20:23], s37 offen lds\n\t"
    "s_add_u32 m0, s53, 45056\n\t"
    "s_nop 0\n\t"
    "buffer_load_dwordx4 v6, s[20:23], s37 offen lds\n\t"
    "s_add_u32 m0, s53, 61440\n\t"
    "s_add_u32 s76, s50, 18432\n\t"
    "s_nop 0\n\t"
    "buffer_load_dwordx4 v1, s[24:27], s76 offen lds\n\t"
    "s_add_u32 m0, s53, 62464\n\t"
    "s_add_u32 s76, s50, 19456\n\t"
    "s_nop 0\n\t"
    "buffer_load_dwordx4 v1, s[24:27], s76 offen lds\n\t"
    "s_add_u32 m0, s53, 63488\n\t"
    "s_add_u32 s76, s50, 20480\n\t"
    "s_nop 0\n\t"
    "buffer_load_dwordx4 v1, s[24:27], s76 offen lds\n\t"
    "s_add_u32 m0, s53, 64512\n\t"
    "s_add_u32 s76, s50, 21504\n\t"
    "s_nop 0\n\t"
    "buffer_load_dwordx4 v1, s[24:27], s76 offen lds\n\t"
    "s_add_u32 m0, s53, 65536\n\t"
    "s_add_u32 s76, s50, 22528\n\t"
    "s_nop 0\n\t"
    "buffer_load_dwordx4 v1, s[24:27], s76 offen lds\n\t"
    "s_mov_b32 s28, s12\n\t"
    "s_and_b32 s29, s13, 0xffff\n\t"
    "s_mov_b32 s30, 28311552\n\t"
    "s_mov_b32 s31, 0x20000\n\t"
    "s_lshr_b32 s59, s58, 1\n\t"
    "s_and_b32 s60, s58, 1\n\t"
    "s_mul_i32 s64, s61, 18432\n\t"
    "s_add_u32 s44, s37, s64\n\t"
    "s_add_u32 s45, s44, s56\n\t"
    "s_add_u32 s46, s45, s56\n\t"
    "s_add_u32 s47, s46, s56\n\t"
    "s_add_u32 s48, s47, s56\n\t"
    "s_add_u32 s49, s48, s56\n\t"
    "v_and_b32 v26, 15, v25\n\t"
    "v_and_b32 v27, 48, v25\n\t"
    "s_mul_i32 s64, s59, 9216\n\t"
    "s_movk_i32 s65, 192\n\t"
    "v_mul_u32_u24 v28, s65, v26\n\t"
    "v_add_u32 v28, s64, v28\n\t"
    "s_lshl_b32 s66, s60, 6\n\t"
    "s_lshl_b32 s67, s60, 5\n\t"
    "s_add_u32 s67, s67, 128\n\t"
    "v_lshrrev_b32 v29, 1, v27\n\t"
    "v_add3_u32 v22, v28, v27, s66\n\t"
    "v_add_u32 v23, v28, v27\n\t"
    "v_add_u32 v23, 128, v23\n\t"
    "s_mov_b32 s98, s60\n\t"
    "v_add_u32 v28, %3, v28\n\t"
    "v_add_u32 v29, 0, v26\n\t"
    "v_lshlrev_b32 v29, 3, v29\n\t"
    "v_and_b32 v29, 32, v29\n\t"
    "v_xor_b32 v29, v29, v27\n\t"
    "v_add3_u32 v7, v28, v29, 0\n\t"
    "v_add_u32 v10, 61440, v7\n\t"
    "v_add_u32 v13, 122880, v7\n\t"
    "v_add_u32 v29, 2, v26\n\t"
    "v_lshlrev_b32 v29, 3, v29\n\t"
    "v_and_b32 v29, 32, v29\n\t"
    "v_xor_b32 v29, v29, v27\n\t"
    "v_add3_u32 v8, v28, v29, 0\n\t"
    "v_add_u32 v8, 384, v8\n\t"
    "v_add_u32 v11, 61440, v8\n\t"
    "v_add_u32 v14, 122880, v8\n\t"
    "v_add_u32 v29, 4, v26\n\t"
    "v_lshlrev_b32 v29, 3, v29\n\t"
    "v_and_b32 v29, 32, v29\n\t"
    "v_xor_b32 v29, v29, v27\n\t"
    "v_add3_u32 v9, v28, v29, 0\n\t"
    "v_add_u32 v9, 768, v9\n\t"
    "v_add_u32 v12, 61440, v9\n\t"
    "v_add_u32 v15, 122880, v9\n\t"
    "v_lshrrev_b32 v28, 1, v27\n\t"
    "v_bfe_u32 v29, v25, 2, 2\n\t"
    "v_add_u32 v28, v28, v29\n\t"
    "v_mul_u32_u24 v28, s65, v28\n\t"
    "v_add_u32 v28, %3, v28\n\t"
    "v_and_b32 v29, 3, v25\n\t"
    "v_lshl_add_u32 v16, v29, 4, v28\n\t"
    "v_add_u32 v16, s66, v16\n\t"
    "v_lshl_add_u32 v17, v29, 3, v28\n\t"
    "v_add_u32 v17, s67, v17\n\t"
    "v_add_u32 v18, 61440, v16\n\t"
    "v_add_u32 v19, 61440, v17\n\t"
    "v_add_u32 v20, 122880, v16\n\t"
    "v_add_u32 v21, 122880, v17\n\t"
    "s_mul_i32 s64, s59, 5120\n\t"
    "s_add_u32 s64, s64, 0\n\t"
    "s_add_u32 s64, s64, %3\n\t"
    "s_movk_i32 s65, 80\n\t"
    "v_mul_u32_u24 v28, s65, v26\n\t"
    "v_add_u32 v28, s64, v28\n\t"
    "v_lshrrev_b32 v30, 1, v27\n\t"
    "s_lshl_b32 s65, s60, 5\n\t"
    "v_add3_u32 v29, v28, v30, s65\n\t"
    "s_mul_i32 s65, s60, 1280\n\t"
    "v_add3_u32 v28, v28, v27, s65\n\t"
    "v_mov_b32 v25, v29\n\t"
    "v_mov_b32 v26, v28\n\t"
    "s_waitcnt vmcnt(10)\n\t"
    "s_barrier\n\t"
    "ds_read_b64_tr_b16 a[0:1], v16 offset:20480\n\t"
    "ds_read_b64_tr_b16 a[2:3], v16 offset:21248\n\t"
    "ds_read_b64_tr_b16 a[4:5], v16 offset:20488\n\t"
    "ds_read_b64_tr_b16 a[6:7], v16 offset:21256\n\t"
    "ds_read_b64_tr_b16 a[8:9], v17 offset:20480\n\t"
    "ds_read_b64_tr_b16 a[10:11], v17 offset:21248\n\t"
    "ds_read_b64_tr_b16 a[12:13], v16 offset:26624\n\t"
    "ds_read_b64_tr_b16 a[14:15], v16 offset:27392\n\t"
    "ds_read_b64_tr_b16 a[16:17], v16 offset:26632\n\t"
    "ds_read_b64_tr_b16 a[18:19], v16 offset:27400\n\t"
    "ds_read_b64_tr_b16 a[20:21], v17 offset:26624\n\t"
    "ds_read_b64_tr_b16 a[22:23], v17 offset:27392\n\t"
    "ds_read_b64_tr_b16 a[24:25], v16 offset:32768\n\t"
    "ds_read_b64_tr_b16 a[26:27], v16 offset:33536\n\t"
    "ds_read_b64_tr_b16 a[28:29], v16 offset:32776\n\t"
    "s_waitcnt lgkmcnt(14)\n\t"
    "ds_read_b64_tr_b16 a[30:31], v16 offset:33544\n\t"
    "s_waitcnt lgkmcnt(14)\n\t"
    "ds_read_b64_tr_b16 a[32:33], v17 offset:32768\n\t"
    "s_waitcnt lgkmcnt(14)\n\t"
    "ds_read_b64_tr_b16 a[34:35], v17 offset:33536\n\t"
    "s_waitcnt lgkmcnt(14)\n\t"
    "ds_read_b128 v[32:35], v7 offset:0\n\t"
    "s_waitcnt lgkmcnt(14)\n\t"
    "ds_read_b128 v[36:39], v7 offset:64\n\t"
    "s_waitcnt lgkmcnt(14)\n\t"
    "ds_read_b128 v[40:43], v7 offset:128\n\t"
    "s_waitcnt lgkmcnt(14)\n\t"
    "ds_read_b128 v[44:47], v7 offset:3072\n\t"
    "s_waitcnt lgkmcnt(14)\n\t"
    "ds_read_b128 v[48:51], v7 offset:3136\n\t"
    "s_waitcnt lgkmcnt(14)\n\t"
    "ds_read_b128 v[52:55], v7 offset:3200\n\t"
    "s_waitcnt lgkmcnt(5)\n\t"
    "v_mfma_f32_16x16x32_bf16 v[64:67], a[0:3], v[32:35], 0\n\t"
    "s_add_u32 m0, s53, 81920\n\t"
    "s_waitcnt lgkmcnt(4)\n\t"
    "v_mfma_f32_16x16x32_bf16 v[64:67], a[12:15], v[36:39], v[64:67]\n\t"
    "buffer_load_dwordx4 v2, s[20:23], s38 offen lds\n\t"
    "s_waitcnt lgkmcnt(3)\n\t"
    "v_mfma_f32_16x16x32_bf16 v[64:67], a[24:27], v[40:43], v[64:67]\n\t"
    "v_mfma_f32_16x16x32_bf16 v[68:71], a[4:7], v[32:35], 0\n\t"
    "ds_read_b128 v[56:59], v7 offset:6144\n\t"
    "v_mfma_f32_16x16x32_bf16 v[68:71], a[16:19], v[36:39], v[68:71]\n\t"
    "ds_read_b128 v[60:63], v7 offset:6208\n\t"
    "v_mfma_f32_16x16x32_bf16 v[68:71], a[28:31], v[40:43], v[68:71]\n\t"
    "s_add_u32 m0, s53, 82944\n\t"
    "v_mfma_f32_16x16x32_bf16 v[72:75], a[8:11], v[32:35], 0\n\t"
    "ds_read_b128 v[32:35], v7 offset:6272\n\t"
    "buffer_load_dwordx4 v3, s[20:23], s38 offen lds\n\t"
    "v_mfma_f32_16x16x32_bf16 v[72:75], a[20:23], v[36:39], v[72:75]\n\t"
    "v_mfma_f32_16x16x32_bf16 v[72:75], a[32:35], v[40:43], v[72:75]\n\t"
    "s_waitcnt lgkmcnt(5)\n\t"
    "v_mfma_f32_16x16x32_bf16 v[76:79], a[0:3], v[44:47], 0\n\t"
    "s_waitcnt lgkmcnt(4)\n\t"
    "v_mfma_f32_16x16x32_bf16 v[76:79], a[12:15], v[48:51], v[76:79]\n\t"
    "s_waitcnt vmcnt(7)\n\t"
    "s_barrier\n\t"
    "s_add_u32 m0, s53, 83968\n\t"
    "s_waitcnt lgkmcnt(3)\n\t"
    "v_mfma_f32_16x16x32_bf16 v[76:79], a[24:27], v[52:55], v[76:79]\n\t"
    "buffer_load_dwordx4 v4, s[20:23], s38 offen lds\n\t"
    "v_mfma_f32_16x16x32_bf16 v[80:83], a[4:7], v[44:47], 0\n\t"
    "ds_read_b128 v[36:39], v7 offset:40960\n\t"
    "v_mfma_f32_16x16x32_bf16 v[80:83], a[16:19], v[48:51], v[80:83]\n\t"
    "ds_read_b128 v[40:43], v7 offset:41024\n\t"
    "v_mfma_f32_16x16x32_bf16 v[80:83], a[28:31], v[52:55], v[80:83]\n\t"
    "v_mfma_f32_16x16x32_bf16 v[84:87], a[8:11], v[44:47], 0\n\t"
    "ds_read_b128 v[44:47], v7 offset:41088\n\t"
    "s_add_u32 m0, s53, 84992\n\t"
    "v_mfma_f32_16x16x32_bf16 v[84:87], a[20:23], v[48:51], v[84:87]\n\t"
    "buffer_load_dwordx4 v5, s[20:23], s38 offen lds\n\t"
    "v_mfma_f32_16x16x32_bf16 v[84:87], a[32:35], v[52:55], v[84:87]\n\t"
    "s_waitcnt lgkmcnt(5)\n\t"
    "v_mfma_f32_16x16x32_bf16 v[88:91], a[0:3], v[56:59], 0\n\t"
    "s_waitcnt lgkmcnt(4)\n\t"
    "v_mfma_f32_16x16x32_bf16 v[88:91], a[12:15], v[60:63], v[88:91]\n\t"
    "s_waitcnt lgkmcnt(3)\n\t"
    "v_mfma_f32_16x16x32_bf16 v[88:91], a[24:27], v[32:35], v[88:91]\n\t"
    "s_add_u32 m0, s53, 86016\n\t"
    "v_mfma_f32_16x16x32_bf16 v[92:95], a[4:7], v[56:59], 0\n\t"
    "ds_read_b128 v[48:51], v7 offset:44032\n\t"
    "buffer_load_dwordx4 v6, s[20:23], s38 offen lds\n\t"
    "v_mfma_f32_16x16x32_bf16 v[92:95], a[16:19], v[60:63], v[92:95]\n\t"
    "ds_read_b128 v[52:55], v7 offset:44096\n\t"
    "s_add_u32 m0, s53, 102400\n\t"
    "s_add_u32 s76, s50, 36864\n\t"
    "v_mfma_f32_16x16x32_bf16 v[92:95], a[28:31], v[32:35], v[92:95]\n\t"
    "buffer_load_dwordx4 v1, s[24:27], s76 offen lds\n\t"
    "v_mfma_f32_16x16x32_bf16 v[96:99], a[8:11], v[56:59], 0\n\t"
    "ds_read_b128 v[56:59], v7 offset:44160\n\t"
    "v_mfma_f32_16x16x32_bf16 v[96:99], a[20:23], v[60:63], v[96:99]\n\t"
    "v_mfma_f32_16x16x32_bf16 v[96:99], a[32:35], v[32:35], v[96:99]\n\t"
    "s_waitcnt lgkmcnt(5)\n\t"
    "v_mfma_f32_16x16x32_bf16 v[100:103], a[0:3], v[36:39], 0\n\t"
    "s_add_u32 m0, s53, 103424\n\t"
    "s_add_u32 s76, s50, 37888\n\t"
    "s_waitcnt lgkmcnt(4)\n\t"
    "v_mfma_f32_16x16x32_bf16 v[100:103], a[12:15], v[40:43], v[100:103]\n\t"
    "buffer_load_dwordx4 v1, s[24:27], s76 offen lds\n\t"
    "s_waitcnt lgkmcnt(3)\n\t"
    "v_mfma_f32_16x16x32_bf16 v[100:103], a[24:27], v[44:47], v[100:103]\n\t"
    "s_waitcnt vmcnt(7)\n\t"
    "s_barrier\n\t"
    "v_mfma_f32_16x16x32_bf16 v[104:107], a[4:7], v[36:39], 0\n\t"
    "ds_read_b64_tr_b16 a[36:37], v18 offset:0\n\t"
    "ds_read_b128 v[60:63], v7 offset:47104\n\t"
    "v_mfma_f32_16x16x32_bf16 v[104:107], a[16:19], v[40:43], v[104:107]\n\t"
    "ds_read_b64_tr_b16 a[38:39], v18 offset:768\n\t"
    "ds_read_b128 v[32:35], v7 offset:47168\n\t"
    "v_mfma_f32_16x16x32_bf16 v[104:107], a[28:31], v[44:47], v[104:107]\n\t"
    "ds_read_b64_tr_b16 a[40:41], v18 offset:8\n\t"
    "s_add_u32 m0, s53, 104448\n\t"
    "s_add_u32 s76, s50, 38912\n\t"
    "v_mfma_f32_16x16x32_bf16 v[108:111], a[8:11], v[36:39], 0\n\t"
    "ds_read_b64_tr_b16 a[42:43], v18 offset:776\n\t"
    "ds_read_b128 v[36:39], v7 offset:47232\n\t"
    "buffer_load_dwordx4 v1, s[24:27], s76 offen lds\n\t"
    "v_mfma_f32_16x16x32_bf16 v[108:111], a[20:23], v[40:43], v[108:111]\n\t"
    "ds_read_b64_tr_b16 a[44:45], v19 offset:0\n\t"
    "v_mfma_f32_16x16x32_bf16 v[108:111], a[32:35], v[44:47], v[108:111]\n\t"
    "ds_read_b64_tr_b16 a[46:47], v19 offset:768\n\t"
    "s_waitcnt lgkmcnt(11)\n\t"
    "v_mfma_f32_16x16x32_bf16 v[112:115], a[0:3], v[48:51], 0\n\t"
    "ds_read_b64_tr_b16 a[48:49], v18 offset:6144\n\t"
    "s_waitcnt lgkmcnt(11)\n\t"
    "v_mfma_f32_16x16x32_bf16 v[112:115], a[12:15], v[52:55], v[112:115]\n\t"
    "ds_read_b64_tr_b16 a[50:51], v18 offset:6912\n\t"
    "s_add_u32 m0, s53, 105472\n\t"
    "s_add_u32 s76, s50, 39936\n\t"
    "s_waitcnt lgkmcnt(11)\n\t"
    "v_mfma_f32_16x16x32_bf16 v[112:115], a[24:27], v[56:59], v[112:115]\n\t"
    "ds_read_b64_tr_b16 a[52:53], v18 offset:6152\n\t"
    "buffer_load_dwordx4 v1, s[24:27], s76 offen lds\n\t"
    "v_mfma_f32_16x16x32_bf16 v[116:119], a[4:7], v[48:51], 0\n\t"
    "ds_read_b64_tr_b16 a[54:55], v18 offset:6920\n\t"
    "ds_read_b128 v[40:43], v8 offset:0\n\t"
    "v_mfma_f32_16x16x32_bf16 v[116:119], a[16:19], v[52:55], v[116:119]\n\t"
    "ds_read_b64_tr_b16 a[56:57], v19 offset:6144\n\t"
    "s_waitcnt lgkmcnt(14)\n\t"
    "ds_read_b128 v[44:47], v8 offset:64\n\t"
    "v_mfma_f32_16x16x32_bf16 v[116:119], a[28:31], v[56:59], v[116:119]\n\t"
    "s_waitcnt lgkmcnt(14)\n\t"
    "ds_read_b64_tr_b16 a[58:59], v19 offset:6912\n\t"
    "v_mfma_f32_16x16x32_bf16 v[120:123], a[8:11], v[48:51], 0\n\t"
    "s_waitcnt lgkmcnt(14)\n\t"
    "ds_read_b64_tr_b16 a[60:61], v18 offset:12288\n\t"
    "s_waitcnt lgkmcnt(14)\n\t"
    "ds_read_b128 v[48:51], v8 offset:128\n\t"
    "s_add_u32 m0, s53, 106496\n\t"
    "s_add_u32 s76, s50, 40960\n\t"
    "v_mfma_f32_16x16x32_bf16 v[120:123], a[20:23], v[52:55], v[120:123]\n\t"
    "s_waitcnt lgkmcnt(14)\n\t"
    "ds_read_b64_tr_b16 a[62:63], v18 offset:13056\n\t"
    "buffer_load_dwordx4 v1, s[24:27], s76 offen lds\n\t"
    "v_mfma_f32_16x16x32_bf16 v[120:123], a[32:35], v[56:59], v[120:123]\n\t"
    "s_waitcnt lgkmcnt(14)\n\t"
    "ds_read_b64_tr_b16 a[64:65], v18 offset:12296\n\t"
    "s_add_u32 m0, s53, 122880\n\t"
    "s_add_u32 s76, s51, 0\n\t"
    "v_mfma_f32_16x16x32_bf16 v[124:127], a[0:3], v[60:63], 0\n\t"
    "s_waitcnt lgkmcnt(14)\n\t"
    "ds_read_b64_tr_b16 a[66:67], v18 offset:13064\n\t"
    "buffer_load_dwordx4 v1, s[24:27], s76 offen lds\n\t"
    "v_mfma_f32_16x16x32_bf16 v[124:127], a[12:15], v[32:35], v[124:127]\n\t"
    "s_waitcnt lgkmcnt(14)\n\t"
    "ds_read_b64_tr_b16 a[68:69], v19 offset:12288\n\t"
    "v_mfma_f32_16x16x32_bf16 v[124:127], a[24:27], v[36:39], v[124:127]\n\t"
    "s_waitcnt lgkmcnt(14)\n\t"
    "ds_read_b64_tr_b16 a[70:71], v19 offset:13056\n\t"
    "v_mfma_f32_16x16x32_bf16 v[128:131], a[4:7], v[60:63], 0\n\t"
    "s_waitcnt lgkmcnt(14)\n\t"
    "ds_read_b128 v[52:55], v8 offset:3072\n\t"
    "v_mfma_f32_16x16x32_bf16 v[128:131], a[16:19], v[32:35], v[128:131]\n\t"
    "s_waitcnt lgkmcnt(14)\n\t"
    "ds_read_b128 v[56:59], v8 offset:3136\n\t"
    "s_add_u32 m0, s53, 123904\n\t"
    "s_add_u32 s76, s51, 1024\n\t"
    "v_mfma_f32_16x16x32_bf16 v[128:131], a[28:31], v[36:39], v[128:131]\n\t"
    "buffer_load_dwordx4 v1, s[24:27], s76 offen lds\n\t"
    "v_mfma_f32_16x16x32_bf16 v[132:135], a[8:11], v[60:63], 0\n\t"
    "s_waitcnt lgkmcnt(14)\n\t"
    "ds_read_b128 v[60:63], v8 offset:3200\n\t"
    "v_mfma_f32_16x16x32_bf16 v[132:135], a[20:23], v[32:35], v[132:135]\n\t"
    "v_mfma_f32_16x16x32_bf16 v[132:135], a[32:35], v[36:39], v[132:135]\n\t"
    "s_waitcnt lgkmcnt(13)\n\t"
    "v_mfma_f32_16x16x32_bf16 v[64:67], a[36:39], v[40:43], v[64:67]\n\t"
    "s_add_u32 m0, s53, 124928\n\t"
    "s_add_u32 s76, s51, 2048\n\t"
    "s_waitcnt lgkmcnt(11)\n\t"
    "v_mfma_f32_16x16x32_bf16 v[64:67], a[48:51], v[44:47], v[64:67]\n\t"
    "buffer_load_dwordx4 v1, s[24:27], s76 offen lds\n\t"
    "s_waitcnt lgkmcnt(7)\n\t"
    "v_mfma_f32_16x16x32_bf16 v[64:67], a[60:63], v[48:51], v[64:67]\n\t"
    "v_mfma_f32_16x16x32_bf16 v[68:71], a[40:43], v[40:43], v[68:71]\n\t"
    "ds_read_b128 v[32:35], v8 offset:6144\n\t"
    "v_mfma_f32_16x16x32_bf16 v[68:71], a[52:55], v[44:47], v[68:71]\n\t"
    "ds_read_b128 v[36:39], v8 offset:6208\n\t"
    "s_waitcnt lgkmcnt(7)\n\t"
    "v_mfma_f32_16x16x32_bf16 v[68:71], a[64:67], v[48:51], v[68:71]\n\t"
    "s_add_u32 m0, s53, 125952\n\t"
    "s_add_u32 s76, s51, 3072\n\t"
    "v_mfma_f32_16x16x32_bf16 v[72:75], a[44:47], v[40:43], v[72:75]\n\t"
    "ds_read_b128 v[40:43], v8 offset:6272\n\t"
    "buffer_load_dwordx4 v1, s[24:27], s76 offen lds\n\t"
    "v_mfma_f32_16x16x32_bf16 v[72:75], a[56:59], v[44:47], v[72:75]\n\t"
    "s_waitcnt lgkmcnt(6)\n\t"
    "v_mfma_f32_16x16x32_bf16 v[72:75], a[68:71], v[48:51], v[72:75]\n\t"
    "s_waitcnt lgkmcnt(5)\n\t"
    "v_mfma_f32_16x16x32_bf16 v[76:79], a[36:39], v[52:55], v[76:79]\n\t"
    "s_waitcnt lgkmcnt(4)\n\t"
    "v_mfma_f32_16x16x32_bf16 v[76:79], a[48:51], v[56:59], v[76:79]\n\t"
    "s_add_u32 m0, s53, 126976\n\t"
    "s_add_u32 s76, s51, 4096\n\t"
    "s_waitcnt lgkmcnt(3)\n\t"
    "v_mfma_f32_16x16x32_bf16 v[76:79], a[60:63], v[60:63], v[76:79]\n\t"
    "buffer_load_dwordx4 v1, s[24:27], s76 offen lds\n\t"
    "v_mfma_f32_16x16x32_bf16 v[80:83], a[40:43], v[52:55], v[80:83]\n\t"
    "ds_read_b128 v[44:47], v8 offset:40960\n\t"
    "s_add_u32 m0, s53, 143360\n\t"
    "s_add_u32 s76, s51, 18432\n\t"
    "v_mfma_f32_16x16x32_bf16 v[80:83], a[52:55], v[56:59], v[80:83]\n\t"
    "ds_read_b128 v[48:51], v8 offset:41024\n\t"
    "buffer_load_dwordx4 v1, s[24:27], s76 offen lds\n\t"
    "v_mfma_f32_16x16x32_bf16 v[80:83], a[64:67], v[60:63], v[80:83]\n\t"
    "v_mfma_f32_16x16x32_bf16 v[84:87], a[44:47], v[52:55], v[84:87]\n\t"
    "ds_read_b128 v[52:55], v8 offset:41088\n\t"
    "v_mfma_f32_16x16x32_bf16 v[84:87], a[56:59], v[56:59], v[84:87]\n\t"
    "v_mfma_f32_16x16x32_bf16 v[84:87], a[68:71], v[60:63], v[84:87]\n\t"
    "s_add_u32 m0, s53, 144384\n\t"
    "s_add_u32 s76, s51, 19456\n\t"
    "s_waitcnt lgkmcnt(5)\n\t"
    "v_mfma_f32_16x16x32_bf16 v[88:91], a[36:39], v[32:35], v[88:91]\n\t"
    "buffer_load_dwordx4 v1, s[24:27], s76 offen lds\n\t"
    "s_waitcnt lgkmcnt(4)\n\t"
    "v_mfma_f32_16x16x32_bf16 v[88:91], a[48:51], v[36:39], v[88:91]\n\t"
    "s_waitcnt lgkmcnt(3)\n\t"
    "v_mfma_f32_16x16x32_bf16 v[88:91], a[60:63], v[40:43], v[88:91]\n\t"
    "v_mfma_f32_16x16x32_bf16 v[92:95], a[40:43], v[32:35], v[92:95]\n\t"
    "ds_read_b128 v[56:59], v8 offset:44032\n\t"
    "v_mfma_f32_16x16x32_bf16 v[92:95], a[52:55], v[36:39], v[92:95]\n\t"
    "ds_read_b128 v[60:63], v8 offset:44096\n\t"
    "s_add_u32 m0, s53, 145408\n\t"
    "s_add_u32 s76, s51, 20480\n\t"
    "v_mfma_f32_16x16x32_bf16 v[92:95], a[64:67], v[40:43], v[92:95]\n\t"
    "buffer_load_dwordx4 v1, s[24:27], s76 offen lds\n\t"
    "v_mfma_f32_16x16x32_bf16 v[96:99], a[44:47], v[32:35], v[96:99]\n\t"
    "ds_read_b128 v[32:35], v8 offset:44160\n\t"
    "v_mfma_f32_16x16x32_bf16 v[96:99], a[56:59], v[36:39], v[96:99]\n\t"
    "v_mfma_f32_16x16x32_bf16 v[96:99], a[68:71], v[40:43], v[96:99]\n\t"
    "s_waitcnt lgkmcnt(5)\n\t"
    "v_mfma_f32_16x16x32_bf16 v[100:103], a[36:39], v[44:47], v[100:103]\n\t"
    "s_add_u32 m0, s53, 146432\n\t"
    "s_add_u32 s76, s51, 21504\n\t"
    "s_waitcnt lgkmcnt(4)\n\t"
    "v_mfma_f32_16x16x32_bf16 v[100:103], a[48:51], v[48:51], v[100:103]\n\t"
    "buffer_load_dwordx4 v1, s[24:27], s76 offen lds\n\t"
    "s_waitcnt lgkmcnt(3)\n\t"
    "v_mfma_f32_16x16x32_bf16 v[100:103], a[60:63], v[52:55], v[100:103]\n\t"
    "v_mfma_f32_16x16x32_bf16 v[104:107], a[40:43], v[44:47], v[104:107]\n\t"
    "ds_read_b128 v[36:39], v8 offset:47104\n\t"
    "v_mfma_f32_16x16x32_bf16 v[104:107], a[52:55], v[48:51], v[104:107]\n\t"
    "ds_read_b128 v[40:43], v8 offset:47168\n\t"
    "v_mfma_f32_16x16x32_bf16 v[104:107], a[64:67], v[52:55], v[104:107]\n\t"
    "s_add_u32 m0, s53, 147456\n\t"
    "s_add_u32 s76, s51, 22528\n\t"
    "v_mfma_f32_16x16x32_bf16 v[108:111], a[44:47], v[44:47], v[108:111]\n\t"
    "ds_read_b128 v[44:47], v8 offset:47232\n\t"
    "buffer_load_dwordx4 v1, s[24:27], s76 offen lds\n\t"
    "v_mfma_f32_16x16x32_bf16 v[108:111], a[56:59], v[48:51], v[108:111]\n\t"
    "s_add_u32 m0, s53, 20480\n\t"
    "s_add_u32 s76, s51, 36864\n\t"
    "v_mfma_f32_16x16x32_bf16 v[108:111], a[68:71], v[52:55], v[108:111]\n\t"
    "buffer_load_dwordx4 v1, s[24:27], s76 offen lds\n\t"
    "s_waitcnt lgkmcnt(5)\n\t"
    "v_mfma_f32_16x16x32_bf16 v[112:115], a[36:39], v[56:59], v[112:115]\n\t"
    "s_waitcnt lgkmcnt(4)\n\t"
    "v_mfma_f32_16x16x32_bf16 v[112:115], a[48:51], v[60:63], v[112:115]\n\t"
    "s_waitcnt vmcnt(16)\n\t"
    "s_barrier\n\t"
    "s_waitcnt lgkmcnt(3)\n\t"
    "v_mfma_f32_16x16x32_bf16 v[112:115], a[60:63], v[32:35], v[112:115]\n\t"
    "v_mfma_f32_16x16x32_bf16 v[116:119], a[40:43], v[56:59], v[116:119]\n\t"
    "ds_read_b128 v[48:51], v10 offset:20480\n\t"
    "s_add_u32 m0, s53, 21504\n\t"
    "s_add_u32 s76, s51, 37888\n\t"
    "v_mfma_f32_16x16x32_bf16 v[116:119], a[52:55], v[60:63], v[116:119]\n\t"
    "ds_read_b128 v[52:55], v10 offset:20544\n\t"
    "buffer_load_dwordx4 v1, s[24:27], s76 offen lds\n\t"
    "v_mfma_f32_16x16x32_bf16 v[116:119], a[64:67], v[32:35], v[116:119]\n\t"
    "v_mfma_f32_16x16x32_bf16 v[120:123], a[44:47], v[56:59], v[120:123]\n\t"
    "ds_read_b128 v[56:59], v10 offset:20608\n\t"
    "v_mfma_f32_16x16x32_bf16 v[120:123], a[56:59], v[60:63], v[120:123]\n\t"
    "v_mfma_f32_16x16x32_bf16 v[120:123], a[68:71], v[32:35], v[120:123]\n\t"
    "s_add_u32 m0, s53, 22528\n\t"
    "s_add_u32 s76, s51, 38912\n\t"
    "s_waitcnt lgkmcnt(5)\n\t"
    "v_mfma_f32_16x16x32_bf16 v[124:127], a[36:39], v[36:39], v[124:127]\n\t"
    "buffer_load_dwordx4 v1, s[24:27], s76 offen lds\n\t"
    "s_waitcnt lgkmcnt(4)\n\t"
    "v_mfma_f32_16x16x32_bf16 v[124:127], a[48:51], v[40:43], v[124:127]\n\t"
    "s_waitcnt lgkmcnt(3)\n\t"
    "v_mfma_f32_16x16x32_bf16 v[124:127], a[60:63], v[44:47], v[124:127]\n\t"
    "v_mfma_f32_16x16x32_bf16 v[128:131], a[40:43], v[36:39], v[128:131]\n\t"
    "ds_read_b128 v[60:63], v10 offset:23552\n\t"
    "v_mfma_f32_16x16x32_bf16 v[128:131], a[52:55], v[40:43], v[128:131]\n\t"
    "ds_read_b128 v[32:35], v10 offset:23616\n\t"
    "s_add_u32 m0, s53, 23552\n\t"
    "s_add_u32 s76, s51, 39936\n\t"
    "v_mfma_f32_16x16x32_bf16 v[128:131], a[64:67], v[44:47], v[128:131]\n\t"
    "buffer_load_dwordx4 v1, s[24:27], s76 offen lds\n\t"
    "v_mfma_f32_16x16x32_bf16 v[132:135], a[44:47], v[36:39], v[132:135]\n\t"
    "ds_read_b128 v[36:39], v10 offset:23680\n\t"
    "v_mfma_f32_16x16x32_bf16 v[132:135], a[56:59], v[40:43], v[132:135]\n\t"
    "v_mfma_f32_16x16x32_bf16 v[132:135], a[68:71], v[44:47], v[132:135]\n\t"
    "s_waitcnt lgkmcnt(5)\n\t"
    "v_mfma_f32_16x16x32_bf16 v[136:139], a[0:3], v[48:51], 0\n\t"
    "s_add_u32 m0, s53, 24576\n\t"
    "s_add_u32 s76, s51, 40960\n\t"
    "s_waitcnt lgkmcnt(4)\n\t"
    "v_mfma_f32_16x16x32_bf16 v[136:139], a[12:15], v[52:55], v[136:139]\n\t"
    "buffer_load_dwordx4 v1, s[24:27], s76 offen lds\n\t"
    "s_waitcnt lgkmcnt(3)\n\t"
    "v_mfma_f32_16x16x32_bf16 v[136:139], a[24:27], v[56:59], v[136:139]\n\t"
    "s_add_u32 m0, s53, 61440\n\t"
    "s_add_u32 s76, s52, 0\n\t"
    "v_mfma_f32_16x16x32_bf16 v[140:143], a[4:7], v[48:51], 0\n\t"
    "ds_read_b128 v[40:43], v10 offset:26624\n\t"
    "buffer_load_dwordx4 v1, s[24:27], s76 offen lds\n\t"
    "v_mfma_f32_16x16x32_bf16 v[140:143], a[16:19], v[52:55], v[140:143]\n\t"
    "ds_read_b128 v[44:47], v10 offset:26688\n\t"
    "v_mfma_f32_16x16x32_bf16 v[140:143], a[28:31], v[56:59], v[140:143]\n\t"
    "v_mfma_f32_16x16x32_bf16 v[144:147], a[8:11], v[48:51], 0\n\t"
    "ds_read_b128 v[48:51], v10 offset:26752\n\t"
    "v_mfma_f32_16x16x32_bf16 v[144:147], a[20:23], v[52:55], v[144:147]\n\t"
    "s_add_u32 m0, s53, 62464\n\t"
    "s_add_u32 s76, s52, 1024\n\t"
    "v_mfma_f32_16x16x32_bf16 v[144:147], a[32:35], v[56:59], v[144:147]\n\t"
    "buffer_load_dwordx4 v1, s[24:27], s76 offen lds\n\t"
    "s_waitcnt lgkmcnt(5)\n\t"
    "v_mfma_f32_16x16x32_bf16 v[148:151], a[0:3], v[60:63], 0\n\t"
    "s_waitcnt lgkmcnt(4)\n\t"
    "v_mfma_f32_16x16x32_bf16 v[148:151], a[12:15], v[32:35], v[148:151]\n\t"
    "s_waitcnt lgkmcnt(3)\n\t"
    "v_mfma_f32_16x16x32_bf16 v[148:151], a[24:27], v[36:39], v[148:151]\n\t"
    "v_mfma_f32_16x16x32_bf16 v[152:155], a[4:7], v[60:63], 0\n\t"
    "ds_read_b128 v[52:55], v11 offset:20480\n\t"
    "s_add_u32 m0, s53, 63488\n\t"
    "s_add_u32 s76, s52, 2048\n\t"
    "v_mfma_f32_16x16x32_bf16 v[152:155], a[16:19], v[32:35], v[152:155]\n\t"
    "ds_read_b128 v[56:59], v11 offset:20544\n\t"
    "buffer_load_dwordx4 v1, s[24:27], s76 offen lds\n\t"
    "v_mfma_f32_16x16x32_bf16 v[152:155], a[28:31], v[36:39], v[152:155]\n\t"
    "v_mfma_f32_16x16x32_bf16 v[156:159], a[8:11], v[60:63], 0\n\t"
    "ds_read_b128 v[60:63], v11 offset:20608\n\t"
    "v_mfma_f32_16x16x32_bf16 v[156:159], a[20:23], v[32:35], v[156:159]\n\t"
    "v_mfma_f32_16x16x32_bf16 v[156:159], a[32:35], v[36:39], v[156:159]\n\t"
    "s_add_u32 m0, s53, 64512\n\t"
    "s_add_u32 s76, s52, 3072\n\t"
    "s_waitcnt lgkmcnt(5)\n\t"
    "v_mfma_f32_16x16x32_bf16 v[160:163], a[0:3], v[40:43], 0\n\t"
    "buffer_load_dwordx4 v1, s[24:27], s76 offen lds\n\t"
    "s_waitcnt lgkmcnt(4)\n\t"
    "v_mfma_f32_16x16x32_bf16 v[160:163], a[12:15], v[44:47], v[160:163]\n\t"
    "s_waitcnt lgkmcnt(3)\n\t"
    "v_mfma_f32_16x16x32_bf16 v[160:163], a[24:27], v[48:51], v[160:163]\n\t"
    "v_mfma_f32_16x16x32_bf16 v[164:167], a[4:7], v[40:43], 0\n\t"
    "ds_read_b128 v[32:35], v11 offset:23552\n\t"
    "v_mfma_f32_16x16x32_bf16 v[164:167], a[16:19], v[44:47], v[164:167]\n\t"
    "ds_read_b128 v[36:39], v11 offset:23616\n\t"
    "s_add_u32 m0, s53, 65536\n\t"
    "s_add_u32 s76, s52, 4096\n\t"
    "v_mfma_f32_16x16x32_bf16 v[164:167], a[28:31], v[48:51], v[164:167]\n\t"
    "buffer_load_dwordx4 v1, s[24:27], s76 offen lds\n\t"
    "v_mfma_f32_16x16x32_bf16 v[168:171], a[8:11], v[40:43], 0\n\t"
    "ds_read_b128 v[40:43], v11 offset:23680\n\t"
    "v_mfma_f32_16x16x32_bf16 v[168:171], a[20:23], v[44:47], v[168:171]\n\t"
    "v_mfma_f32_16x16x32_bf16 v[168:171], a[32:35], v[48:51], v[168:171]\n\t"
    "s_waitcnt lgkmcnt(5)\n\t"
    "v_mfma_f32_16x16x32_bf16 v[136:139], a[36:39], v[52:55], v[136:139]\n\t"
    "s_waitcnt lgkmcnt(4)\n\t"
    "v_mfma_f32_16x16x32_bf16 v[136:139], a[48:51], v[56:59], v[136:139]\n\t"
    "s_waitcnt lgkmcnt(3)\n\t"
    "v_mfma_f32_16x16x32_bf16 v[136:139], a[60:63], v[60:63], v[136:139]\n\t"
    "s_waitcnt vmcnt(20)\n\t"
    "s_barrier\n\t"
    "v_mfma_f32_16x16x32_bf16 v[140:143], a[40:43], v[52:55], v[140:143]\n\t"
    "ds_read_b64_tr_b16 a[72:73], v18 offset:40960\n\t"
    "ds_read_b128 v[44:47], v11 offset:26624\n\t"
    "v_mfma_f32_16x16x32_bf16 v[140:143], a[52:55], v[56:59], v[140:143]\n\t"
    "ds_read_b64_tr_b16 a[74:75], v18 offset:41728\n\t"
    "ds_read_b128 v[48:51], v11 offset:26688\n\t"
    "v_mfma_f32_16x16x32_bf16 v[140:143], a[64:67], v[60:63], v[140:143]\n\t"
    "ds_read_b64_tr_b16 a[76:77], v18 offset:40968\n\t"
    "v_mfma_f32_16x16x32_bf16 v[144:147], a[44:47], v[52:55], v[144:147]\n\t"
    "ds_read_b64_tr_b16 a[78:79], v18 offset:41736\n\t"
    "ds_read_b128 v[52:55], v11 offset:26752\n\t"
    "v_mfma_f32_16x16x32_bf16 v[144:147], a[56:59], v[56:59], v[144:147]\n\t"
    "ds_read_b64_tr_b16 a[80:81], v19 offset:40960\n\t"
    "v_mfma_f32_16x16x32_bf16 v[144:147], a[68:71], v[60:63], v[144:147]\n\t"
    "ds_read_b64_tr_b16 a[82:83], v19 offset:41728\n\t"
    "s_waitcnt lgkmcnt(11)\n\t"
    "v_mfma_f32_16x16x32_bf16 v[148:151], a[36:39], v[32:35], v[148:151]\n\t"
    "ds_read_b64_tr_b16 a[84:85], v18 offset:47104\n\t"
    "s_waitcnt lgkmcnt(11)\n\t"
    "v_mfma_f32_16x16x32_bf16 v[148:151], a[48:51], v[36:39], v[148:151]\n\t"
    "ds_read_b64_tr_b16 a[86:87], v18 offset:47872\n\t"
    "s_waitcnt lgkmcnt(11)\n\t"
    "v_mfma_f32_16x16x32_bf16 v[148:151], a[60:63], v[40:43], v[148:151]\n\t"
    "ds_read_b64_tr_b16 a[88:89], v18 offset:47112\n\t"
    "v_mfma_f32_16x16x32_bf16 v[152:155], a[40:43], v[32:35], v[152:155]\n\t"
    "ds_read_b64_tr_b16 a[90:91], v18 offset:47880\n\t"
    "ds_read_b128 v[56:59], v9 offset:0\n\t"
    "v_mfma_f32_16x16x32_bf16 v[152:155], a[52:55], v[36:39], v[152:155]\n\t"
    "ds_read_b64_tr_b16 a[92:93], v19 offset:47104\n\t"
    "s_waitcnt lgkmcnt(14)\n\t"
    "ds_read_b128 v[60:63], v9 offset:64\n\t"
    "v_mfma_f32_16x16x32_bf16 v[152:155], a[64:67], v[40:43], v[152:155]\n\t"
    "s_waitcnt lgkmcnt(14)\n\t"
    "ds_read_b64_tr_b16 a[94:95], v19 offset:47872\n\t"
    "v_mfma_f32_16x16x32_bf16 v[156:159], a[44:47], v[32:35], v[156:159]\n\t"
    "s_waitcnt lgkmcnt(14)\n\t"
    "ds_read_b64_tr_b16 a[96:97], v18 offset:53248\n\t"
    "s_waitcnt lgkmcnt(14)\n\t"
    "ds_read_b128 v[32:35], v9 offset:128\n\t"
    "v_mfma_f32_16x16x32_bf16 v[156:159], a[56:59], v[36:39], v[156:159]\n\t"
    "s_waitcnt lgkmcnt(14)\n\t"
    "ds_read_b64_tr_b16 a[98:99], v18 offset:54016\n\t"
    "v_mfma_f32_16x16x32_bf16 v[156:159], a[68:71], v[40:43], v[156:159]\n\t"
    "s_waitcnt lgkmcnt(14)\n\t"
    "ds_read_b64_tr_b16 a[100:101], v18 offset:53256\n\t"
    "v_mfma_f32_16x16x32_bf16 v[160:163], a[36:39], v[44:47], v[160:163]\n\t"
    "s_waitcnt lgkmcnt(14)\n\t"
    "ds_read_b64_tr_b16 a[102:103], v18 offset:54024\n\t"
    "v_mfma_f32_16x16x32_bf16 v[160:163], a[48:51], v[48:51], v[160:163]\n\t"
    "s_waitcnt lgkmcnt(14)\n\t"
    "ds_read_b64_tr_b16 a[104:105], v19 offset:53248\n\t"
    "v_mfma_f32_16x16x32_bf16 v[160:163], a[60:63], v[52:55], v[160:163]\n\t"
    "s_waitcnt lgkmcnt(14)\n\t"
    "ds_read_b64_tr_b16 a[106:107], v19 offset:54016\n\t"
    "v_mfma_f32_16x16x32_bf16 v[164:167], a[40:43], v[44:47], v[164:167]\n\t"
    "s_waitcnt lgkmcnt(14)\n\t"
    "ds_read_b128 v[36:39], v9 offset:3072\n\t"
    "v_mfma_f32_16x16x32_bf16 v[164:167], a[52:55], v[48:51], v[164:167]\n\t"
    "s_waitcnt lgkmcnt(14)\n\t"
    "ds_read_b128 v[40:43], v9 offset:3136\n\t"
    "v_mfma_f32_16x16x32_bf16 v[164:167], a[64:67], v[52:55], v[164:167]\n\t"
    "v_mfma_f32_16x16x32_bf16 v[168:171], a[44:47], v[44:47], v[168:171]\n\t"
    "s_waitcnt lgkmcnt(14)\n\t"
    "ds_read_b128 v[44:47], v9 offset:3200\n\t"
    "v_mfma_f32_16x16x32_bf16 v[168:171], a[56:59], v[48:51], v[168:171]\n\t"
    "v_mfma_f32_16x16x32_bf16 v[168:171], a[68:71], v[52:55], v[168:171]\n\t"
    "s_waitcnt lgkmcnt(13)\n\t"
    "v_mfma_f32_16x16x32_bf16 v[64:67], a[72:75], v[56:59], v[64:67]\n\t"
    "s_waitcnt lgkmcnt(11)\n\t"
    "v_mfma_f32_16x16x32_bf16 v[64:67], a[84:87], v[60:63], v[64:67]\n\t"
    "s_waitcnt lgkmcnt(7)\n\t"
    "v_mfma_f32_16x16x32_bf16 v[64:67], a[96:99], v[32:35], v[64:67]\n\t"
    "v_mfma_f32_16x16x32_bf16 v[68:71], a[76:79], v[56:59], v[68:71]\n\t"
    "ds_read_b128 v[48:51], v9 offset:6144\n\t"
    "v_mfma_f32_16x16x32_bf16 v[68:71], a[88:91], v[60:63], v[68:71]\n\t"
    "ds_read_b128 v[52:55], v9 offset:6208\n\t"
    "s_waitcnt lgkmcnt(7)\n\t"
    "v_mfma_f32_16x16x32_bf16 v[68:71], a[100:103], v[32:35], v[68:71]\n\t"
    "v_mfma_f32_16x16x32_bf16 v[72:75], a[80:83], v[56:59], v[72:75]\n\t"
    "ds_read_b128 v[56:59], v9 offset:6272\n\t"
    "v_mfma_f32_16x16x32_bf16 v[72:75], a[92:95], v[60:63], v[72:75]\n\t"
    "s_waitcnt lgkmcnt(6)\n\t"
    "v_mfma_f32_16x16x32_bf16 v[72:75], a[104:107], v[32:35], v[72:75]\n\t"
    "s_waitcnt lgkmcnt(5)\n\t"
    "v_mfma_f32_16x16x32_bf16 v[76:79], a[72:75], v[36:39], v[76:79]\n\t"
    "s_waitcnt lgkmcnt(4)\n\t"
    "v_mfma_f32_16x16x32_bf16 v[76:79], a[84:87], v[40:43], v[76:79]\n\t"
    "s_waitcnt lgkmcnt(3)\n\t"
    "v_mfma_f32_16x16x32_bf16 v[76:79], a[96:99], v[44:47], v[76:79]\n\t"
    "v_mfma_f32_16x16x32_bf16 v[80:83], a[76:79], v[36:39], v[80:83]\n\t"
    "ds_read_b128 v[60:63], v9 offset:40960\n\t"
    "v_mfma_f32_16x16x32_bf16 v[80:83], a[88:91], v[40:43], v[80:83]\n\t"
    "ds_read_b128 v[32:35], v9 offset:41024\n\t"
    "v_mfma_f32_16x16x32_bf16 v[80:83], a[100:103], v[44:47], v[80:83]\n\t"
    "v_mfma_f32_16x16x32_bf16 v[84:87], a[80:83], v[36:39], v[84:87]\n\t"
    "ds_read_b128 v[36:39], v9 offset:41088\n\t"
    "v_mfma_f32_16x16x32_bf16 v[84:87], a[92:95], v[40:43], v[84:87]\n\t"
    "v_mfma_f32_16x16x32_bf16 v[84:87], a[104:107], v[44:47], v[84:87]\n\t"
    "s_waitcnt lgkmcnt(5)\n\t"
    "v_mfma_f32_16x16x32_bf16 v[88:91], a[72:75], v[48:51], v[88:91]\n\t"
    "s_waitcnt lgkmcnt(4)\n\t"
    "v_mfma_f32_16x16x32_bf16 v[88:91], a[84:87], v[52:55], v[88:91]\n\t"
    "s_waitcnt lgkmcnt(3)\n\t"
    "v_mfma_f32_16x16x32_bf16 v[88:91], a[96:99], v[56:59], v[88:91]\n\t"
    "v_mfma_f32_16x16x32_bf16 v[92:95], a[76:79], v[48:51], v[92:95]\n\t"
    "ds_read_b128 v[40:43], v9 offset:44032\n\t"
    "v_mfma_f32_16x16x32_bf16 v[92:95], a[88:91], v[52:55], v[92:95]\n\t"
    "ds_read_b128 v[44:47], v9 offset:44096\n\t"
    "v_mfma_f32_16x16x32_bf16 v[92:95], a[100:103], v[56:59], v[92:95]\n\t"
    "v_mfma_f32_16x16x32_bf16 v[96:99], a[80:83], v[48:51], v[96:99]\n\t"
    "ds_read_b128 v[48:51], v9 offset:44160\n\t"
    "v_mfma_f32_16x16x32_bf16 v[96:99], a[92:95], v[52:55], v[96:99]\n\t"
    "v_mfma_f32_16x16x32_bf16 v[96:99], a[104:107], v[56:59], v[96:99]\n\t"
    "s_waitcnt lgkmcnt(5)\n\t"
    "v_mfma_f32_16x16x32_bf16 v[100:103], a[72:75], v[60:63], v[100:103]\n\t"
    "s_waitcnt lgkmcnt(4)\n\t"
    "v_mfma_f32_16x16x32_bf16 v[100:103], a[84:87], v[32:35], v[100:103]\n\t"
    "s_waitcnt lgkmcnt(3)\n\t"
    "v_mfma_f32_16x16x32_bf16 v[100:103], a[96:99], v[36:39], v[100:103]\n\t"
    "v_mfma_f32_16x16x32_bf16 v[104:107], a[76:79], v[60:63], v[104:107]\n\t"
    "ds_read_b128 v[52:55], v9 offset:47104\n\t"
    "v_mfma_f32_16x16x32_bf16 v[104:107], a[88:91], v[32:35], v[104:107]\n\t"
    "ds_read_b128 v[56:59], v9 offset:47168\n\t"
    "v_mfma_f32_16x16x32_bf16 v[104:107], a[100:103], v[36:39], v[104:107]\n\t"
    "v_mfma_f32_16x16x32_bf16 v[108:111], a[80:83], v[60:63], v[108:111]\n\t"
    "ds_read_b128 v[60:63], v9 offset:47232\n\t"
    "v_mfma_f32_16x16x32_bf16 v[108:111], a[92:95], v[32:35], v[108:111]\n\t"
    "v_mfma_f32_16x16x32_bf16 v[108:111], a[104:107], v[36:39], v[108:111]\n\t"
    "s_waitcnt lgkmcnt(5)\n\t"
    "v_mfma_f32_16x16x32_bf16 v[112:115], a[72:75], v[40:43], v[112:115]\n\t"
    "s_waitcnt lgkmcnt(4)\n\t"
    "v_mfma_f32_16x16x32_bf16 v[112:115], a[84:87], v[44:47], v[112:115]\n\t"
    "s_waitcnt lgkmcnt(3)\n\t"
    "v_mfma_f32_16x16x32_bf16 v[112:115], a[96:99], v[48:51], v[112:115]\n\t"
    "v_mfma_f32_16x16x32_bf16 v[116:119], a[76:79], v[40:43], v[116:119]\n\t"
    "ds_read_b128 v[32:35], v12 offset:20480\n\t"
    "v_mfma_f32_16x16x32_bf16 v[116:119], a[88:91], v[44:47], v[116:119]\n\t"
    "ds_read_b128 v[36:39], v12 offset:20544\n\t"
    "v_mfma_f32_16x16x32_bf16 v[116:119], a[100:103], v[48:51], v[116:119]\n\t"
    "v_mfma_f32_16x16x32_bf16 v[120:123], a[80:83], v[40:43], v[120:123]\n\t"
    "ds_read_b128 v[40:43], v12 offset:20608\n\t"
    "v_mfma_f32_16x16x32_bf16 v[120:123], a[92:95], v[44:47], v[120:123]\n\t"
    "v_mfma_f32_16x16x32_bf16 v[120:123], a[104:107], v[48:51], v[120:123]\n\t"
    "s_waitcnt lgkmcnt(5)\n\t"
    "v_mfma_f32_16x16x32_bf16 v[124:127], a[72:75], v[52:55], v[124:127]\n\t"
    "s_waitcnt lgkmcnt(4)\n\t"
    "v_mfma_f32_16x16x32_bf16 v[124:127], a[84:87], v[56:59], v[124:127]\n\t"
    "s_waitcnt lgkmcnt(3)\n\t"
    "v_mfma_f32_16x16x32_bf16 v[124:127], a[96:99], v[60:63], v[124:127]\n\t"
    "v_mfma_f32_16x16x32_bf16 v[128:131], a[76:79], v[52:55], v[128:131]\n\t"
    "ds_read_b128 v[44:47], v12 offset:23552\n\t"
    "v_mfma_f32_16x16x32_bf16 v[128:131], a[88:91], v[56:59], v[128:131]\n\t"
    "ds_read_b128 v[48:51], v12 offset:23616\n\t"
    "v_mfma_f32_16x16x32_bf16 v[128:131], a[100:103], v[60:63], v[128:131]\n\t"
    "v_mfma_f32_16x16x32_bf16 v[132:135], a[80:83], v[52:55], v[132:135]\n\t"
    "ds_read_b128 v[52:55], v12 offset:23680\n\t"
    "v_mfma_f32_16x16x32_bf16 v[132:135], a[92:95], v[56:59], v[132:135]\n\t"
    "v_mfma_f32_16x16x32_bf16 v[132:135], a[104:107], v[60:63], v[132:135]\n\t"
    "s_waitcnt lgkmcnt(5)\n\t"
    "v_mfma_f32_16x16x32_bf16 v[136:139], a[72:75], v[32:35], v[136:139]\n\t"
    "s_waitcnt lgkmcnt(4)\n\t"
    "v_mfma_f32_16x16x32_bf16 v[136:139], a[84:87], v[36:39], v[136:139]\n\t"
    "s_waitcnt lgkmcnt(3)\n\t"
    "v_mfma_f32_16x16x32_bf16 v[136:139], a[96:99], v[40:43], v[136:139]\n\t"
    "s_waitcnt vmcnt(15)\n\t"
    "s_barrier\n\t"
    "v_mfma_f32_16x16x32_bf16 v[140:143], a[76:79], v[32:35], v[140:143]\n\t"
    "ds_read_b64_tr_b16 a[108:109], v20 offset:0\n\t"
    "ds_read_b128 v[56:59], v12 offset:26624\n\t"
    "s_add_u32 m0, s53, 0\n\t"
    "s_add_u32 s76, s52, 18432\n\t"
    "v_mfma_f32_16x16x32_bf16 v[140:143], a[88:91], v[36:39], v[140:143]\n\t"
    "ds_read_b64_tr_b16 a[110:111], v20 offset:768\n\t"
    "ds_read_b128 v[60:63], v12 offset:26688\n\t"
    "buffer_load_dwordx4 v1, s[24:27], s76 offen lds\n\t"
    "v_mfma_f32_16x16x32_bf16 v[140:143], a[100:103], v[40:43], v[140:143]\n\t"
    "ds_read_b64_tr_b16 a[112:113], v20 offset:8\n\t"
    "v_mfma_f32_16x16x32_bf16 v[144:147], a[80:83], v[32:35], v[144:147]\n\t"
    "ds_read_b64_tr_b16 a[114:115], v20 offset:776\n\t"
    "ds_read_b128 v[32:35], v12 offset:26752\n\t"
    "v_mfma_f32_16x16x32_bf16 v[144:147], a[92:95], v[36:39], v[144:147]\n\t"
    "ds_read_b64_tr_b16 a[116:117], v21 offset:0\n\t"
    "v_mfma_f32_16x16x32_bf16 v[144:147], a[104:107], v[40:43], v[144:147]\n\t"
    "ds_read_b64_tr_b16 a[118:119], v21 offset:768\n\t"
    "s_add_u32 m0, s53, 1024\n\t"
    "s_add_u32 s76, s52, 19456\n\t"
    "s_waitcnt lgkmcnt(11)\n\t"
    "v_mfma_f32_16x16x32_bf16 v[148:151], a[72:75], v[44:47], v[148:151]\n\t"
    "ds_read_b64_tr_b16 a[120:121], v20 offset:6144\n\t"
    "buffer_load_dwordx4 v1, s[24:27], s76 offen lds\n\t"
    "s_waitcnt lgkmcnt(11)\n\t"
    "v_mfma_f32_16x16x32_bf16 v[148:151], a[84:87], v[48:51], v[148:151]\n\t"
    "ds_read_b64_tr_b16 a[122:123], v20 offset:6912\n\t"
    "s_waitcnt lgkmcnt(11)\n\t"
    "v_mfma_f32_16x16x32_bf16 v[148:151], a[96:99], v[52:55], v[148:151]\n\t"
    "ds_read_b64_tr_b16 a[124:125], v20 offset:6152\n\t"
    "v_mfma_f32_16x16x32_bf16 v[152:155], a[76:79], v[44:47], v[152:155]\n\t"
    "ds_read_b64_tr_b16 a[126:127], v20 offset:6920\n\t"
    "ds_read_b128 v[36:39], v7 offset:40960\n\t"
    "v_mfma_f32_16x16x32_bf16 v[152:155], a[88:91], v[48:51], v[152:155]\n\t"
    "ds_read_b64_tr_b16 a[128:129], v21 offset:6144\n\t"
    "s_waitcnt lgkmcnt(14)\n\t"
    "ds_read_b128 v[40:43], v7 offset:41024\n\t"
    "s_add_u32 m0, s53, 2048\n\t"
    "s_add_u32 s76, s52, 20480\n\t"
    "v_mfma_f32_16x16x32_bf16 v[152:155], a[100:103], v[52:55], v[152:155]\n\t"
    "s_waitcnt lgkmcnt(14)\n\t"
    "ds_read_b64_tr_b16 a[130:131], v21 offset:6912\n\t"
    "buffer_load_dwordx4 v1, s[24:27], s76 offen lds\n\t"
    "v_mfma_f32_16x16x32_bf16 v[156:159], a[80:83], v[44:47], v[156:159]\n\t"
    "s_waitcnt lgkmcnt(14)\n\t"
    "ds_read_b64_tr_b16 a[132:133], v20 offset:12288\n\t"
    "s_waitcnt lgkmcnt(14)\n\t"
    "ds_read_b128 v[44:47], v7 offset:41088\n\t"
    "v_mfma_f32_16x16x32_bf16 v[156:159], a[92:95], v[48:51], v[156:159]\n\t"
    "s_waitcnt lgkmcnt(14)\n\t"
    "ds_read_b64_tr_b16 a[134:135], v20 offset:13056\n\t"
    "v_mfma_f32_16x16x32_bf16 v[156:159], a[104:107], v[52:55], v[156:159]\n\t"
    "s_waitcnt lgkmcnt(14)\n\t"
    "ds_read_b64_tr_b16 a[136:137], v20 offset:12296\n\t"
    "v_mfma_f32_16x16x32_bf16 v[160:163], a[72:75], v[56:59], v[160:163]\n\t"
    "s_waitcnt lgkmcnt(14)\n\t"
    "ds_read_b64_tr_b16 a[138:139], v20 offset:13064\n\t"
    "s_add_u32 m0, s53, 3072\n\t"
    "s_add_u32 s76, s52, 21504\n\t"
    "v_mfma_f32_16x16x32_bf16 v[160:163], a[84:87], v[60:63], v[160:163]\n\t"
    "s_waitcnt lgkmcnt(14)\n\t"
    "ds_read_b64_tr_b16 a[140:141], v21 offset:12288\n\t"
    "buffer_load_dwordx4 v1, s[24:27], s76 offen lds\n\t"
    "v_mfma_f32_16x16x32_bf16 v[160:163], a[96:99], v[32:35], v[160:163]\n\t"
    "s_waitcnt lgkmcnt(14)\n\t"
    "ds_read_b64_tr_b16 a[142:143], v21 offset:13056\n\t"
    "v_mfma_f32_16x16x32_bf16 v[164:167], a[76:79], v[56:59], v[164:167]\n\t"
    "s_waitcnt lgkmcnt(14)\n\t"
    "ds_read_b128 v[48:51], v7 offset:44032\n\t"
    "v_mfma_f32_16x16x32_bf16 v[164:167], a[88:91], v[60:63], v[164:167]\n\t"
    "s_waitcnt lgkmcnt(14)\n\t"
    "ds_read_b128 v[52:55], v7 offset:44096\n\t"
    "v_mfma_f32_16x16x32_bf16 v[164:167], a[100:103], v[32:35], v[164:167]\n\t"
    "s_add_u32 m0, s53, 4096\n\t"
    "s_add_u32 s76, s52, 22528\n\t"
    "v_mfma_f32_16x16x32_bf16 v[168:171], a[80:83], v[56:59], v[168:171]\n\t"
    "s_waitcnt lgkmcnt(14)\n\t"
    "ds_read_b128 v[56:59], v7 offset:44160\n\t"
    "buffer_load_dwordx4 v1, s[24:27], s76 offen lds\n\t"
    "v_mfma_f32_16x16x32_bf16 v[168:171], a[92:95], v[60:63], v[168:171]\n\t"
    "s_add_u32 m0, s53, 102400\n\t"
    "s_add_u32 s76, s52, 36864\n\t"
    "v_mfma_f32_16x16x32_bf16 v[168:171], a[104:107], v[32:35], v[168:171]\n\t"
    "buffer_load_dwordx4 v1, s[24:27], s76 offen lds\n\t"
    "s_waitcnt lgkmcnt(13)\n\t"
    "v_mfma_f32_16x16x32_bf16 v[64:67], a[108:111], v[36:39], v[64:67]\n\t"
    "s_waitcnt lgkmcnt(11)\n\t"
    "v_mfma_f32_16x16x32_bf16 v[64:67], a[120:123], v[40:43], v[64:67]\n\t"
    "s_waitcnt lgkmcnt(7)\n\t"
    "v_mfma_f32_16x16x32_bf16 v[64:67], a[132:135], v[44:47], v[64:67]\n\t"
    "s_waitcnt vmcnt(16)\n\t"
    "s_waitcnt lgkmcnt(3)\n\t"
    "s_barrier\n\t"
    "v_mfma_f32_16x16x32_bf16 v[68:71], a[112:115], v[36:39], v[68:71]\n\t"
    "ds_read_b64_tr_b16 a[144:145], v20 offset:20480\n\t"
    "ds_read_b128 v[60:63], v7 offset:47104\n\t"
    "s_add_u32 m0, s53, 103424\n\t"
    "s_add_u32 s76, s52, 37888\n\t"
    "v_mfma_f32_16x16x32_bf16 v[68:71], a[124:127], v[40:43], v[68:71]\n\t"
    "ds_read_b64_tr_b16 a[146:147], v20 offset:21248\n\t"
    "ds_read_b128 v[32:35], v7 offset:47168\n\t"
    "buffer_load_dwordx4 v1, s[24:27], s76 offen lds\n\t"
    "v_mfma_f32_16x16x32_bf16 v[68:71], a[136:139], v[44:47], v[68:71]\n\t"
    "ds_read_b64_tr_b16 a[148:149], v20 offset:20488\n\t"
    "v_mfma_f32_16x16x32_bf16 v[72:75], a[116:119], v[36:39], v[72:75]\n\t"
    "ds_read_b64_tr_b16 a[150:151], v20 offset:21256\n\t"
    "ds_read_b128 v[36:39], v7 offset:47232\n\t"
    "v_mfma_f32_16x16x32_bf16 v[72:75], a[128:131], v[40:43], v[72:75]\n\t"
    "ds_read_b64_tr_b16 a[152:153], v21 offset:20480\n\t"
    "v_mfma_f32_16x16x32_bf16 v[72:75], a[140:143], v[44:47], v[72:75]\n\t"
    "ds_read_b64_tr_b16 a[154:155], v21 offset:21248\n\t"
    "s_add_u32 m0, s53, 104448\n\t"
    "s_add_u32 s76, s52, 38912\n\t"
    "s_waitcnt lgkmcnt(11)\n\t"
    "v_mfma_f32_16x16x32_bf16 v[76:79], a[108:111], v[48:51], v[76:79]\n\t"
    "ds_read_b64_tr_b16 a[156:157], v20 offset:26624\n\t"
    "buffer_load_dwordx4 v1, s[24:27], s76 offen lds\n\t"
    "s_waitcnt lgkmcnt(11)\n\t"
    "v_mfma_f32_16x16x32_bf16 v[76:79], a[120:123], v[52:55], v[76:79]\n\t"
    "ds_read_b64_tr_b16 a[158:159], v20 offset:27392\n\t"
    "s_waitcnt lgkmcnt(11)\n\t"
    "v_mfma_f32_16x16x32_bf16 v[76:79], a[132:135], v[56:59], v[76:79]\n\t"
    "ds_read_b64_tr_b16 a[160:161], v20 offset:26632\n\t"
    "v_mfma_f32_16x16x32_bf16 v[80:83], a[112:115], v[48:51], v[80:83]\n\t"
    "ds_read_b64_tr_b16 a[162:163], v20 offset:27400\n\t"
    "ds_read_b128 v[40:43], v8 offset:40960\n\t"
    "v_mfma_f32_16x16x32_bf16 v[80:83], a[124:127], v[52:55], v[80:83]\n\t"
    "ds_read_b64_tr_b16 a[164:165], v21 offset:26624\n\t"
    "s_waitcnt lgkmcnt(14)\n\t"
    "ds_read_b128 v[44:47], v8 offset:41024\n\t"
    "s_add_u32 m0, s53, 105472\n\t"
    "s_add_u32 s76, s52, 39936\n\t"
    "v_mfma_f32_16x16x32_bf16 v[80:83], a[136:139], v[56:59], v[80:83]\n\t"
    "s_waitcnt lgkmcnt(14)\n\t"
    "ds_read_b64_tr_b16 a[166:167], v21 offset:27392\n\t"
    "buffer_load_dwordx4 v1, s[24:27], s76 offen lds\n\t"
    "v_mfma_f32_16x16x32_bf16 v[84:87], a[116:119], v[48:51], v[84:87]\n\t"
    "s_waitcnt lgkmcnt(14)\n\t"
    "ds_read_b64_tr_b16 a[168:169], v20 offset:32768\n\t"
    "s_waitcnt lgkmcnt(14)\n\t"
    "ds_read_b128 v[48:51], v8 offset:41088\n\t"
    "v_mfma_f32_16x16x32_bf16 v[84:87], a[128:131], v[52:55], v[84:87]\n\t"
    "s_waitcnt lgkmcnt(14)\n\t"
    "ds_read_b64_tr_b16 a[170:171], v20 offset:33536\n\t"
    "v_mfma_f32_16x16x32_bf16 v[84:87], a[140:143], v[56:59], v[84:87]\n\t"
    "s_waitcnt lgkmcnt(14)\n\t"
    "ds_read_b64_tr_b16 a[172:173], v20 offset:32776\n\t"
    "v_mfma_f32_16x16x32_bf16 v[88:91], a[108:111], v[60:63], v[88:91]\n\t"
    "s_waitcnt lgkmcnt(14)\n\t"
    "ds_read_b64_tr_b16 a[174:175], v20 offset:33544\n\t"
    "s_add_u32 m0, s53, 106496\n\t"
    "s_add_u32 s76, s52, 40960\n\t"
    "v_mfma_f32_16x16x32_bf16 v[88:91], a[120:123], v[32:35], v[88:91]\n\t"
    "s_waitcnt lgkmcnt(14)\n\t"
    "ds_read_b64_tr_b16 a[176:177], v21 offset:32768\n\t"
    "buffer_load_dwordx4 v1, s[24:27], s76 offen lds\n\t"
    "v_mfma_f32_16x16x32_bf16 v[88:91], a[132:135], v[36:39], v[88:91]\n\t"
    "s_waitcnt lgkmcnt(14)\n\t"
    "ds_read_b64_tr_b16 a[178:179], v21 offset:33536\n\t"
    "s_add_u32 m0, s53, 122880\n\t"
    "v_mfma_f32_16x16x32_bf16 v[92:95], a[112:115], v[60:63], v[92:95]\n\t"
    "s_waitcnt lgkmcnt(14)\n\t"
    "ds_read_b128 v[52:55], v8 offset:44032\n\t"
    "buffer_load_dwordx4 v2, s[20:23], s39 offen lds\n\t"
    "v_mfma_f32_16x16x32_bf16 v[92:95], a[124:127], v[32:35], v[92:95]\n\t"
    "s_waitcnt lgkmcnt(14)\n\t"
    "ds_read_b128 v[56:59], v8 offset:44096\n\t"
    "v_mfma_f32_16x16x32_bf16 v[92:95], a[136:139], v[36:39], v[92:95]\n\t"
    "v_mfma_f32_16x16x32_bf16 v[96:99], a[116:119], v[60:63], v[96:99]\n\t"
    "s_waitcnt lgkmcnt(14)\n\t"
    "ds_read_b128 v[60:63], v8 offset:44160\n\t"
    "v_mfma_f32_16x16x32_bf16 v[96:99], a[128:131], v[32:35], v[96:99]\n\t"
    "s_add_u32 m0, s53, 123904\n\t"
    "v_mfma_f32_16x16x32_bf16 v[96:99], a[140:143], v[36:39], v[96:99]\n\t"
    "buffer_load_dwordx4 v3, s[20:23], s39 offen lds\n\t"
    "s_waitcnt lgkmcnt(13)\n\t"
    "v_mfma_f32_16x16x32_bf16 v[64:67], a[144:147], v[40:43], v[64:67]\n\t"
    "s_waitcnt lgkmcnt(11)\n\t"
    "v_mfma_f32_16x16x32_bf16 v[64:67], a[156:159], v[44:47], v[64:67]\n\t"
    "s_waitcnt lgkmcnt(7)\n\t"
    "v_mfma_f32_16x16x32_bf16 v[64:67], a[168:171], v[48:51], v[64:67]\n\t"
    "s_waitcnt vmcnt(17)\n\t"
    "s_waitcnt lgkmcnt(3)\n\t"
    "s_barrier\n\t"
    "v_mfma_f32_16x16x32_bf16 v[68:71], a[148:151], v[40:43], v[68:71]\n\t"
    "ds_read_b64_tr_b16 a[180:181], v16 offset:20480\n\t"
    "ds_read_b128 v[32:35], v8 offset:47104\n\t"
    "s_add_u32 m0, s53, 124928\n\t"
    "v_mfma_f32_16x16x32_bf16 v[68:71], a[160:163], v[44:47], v[68:71]\n\t"
    "ds_read_b64_tr_b16 a[182:183], v16 offset:21248\n\t"
    "ds_read_b128 v[36:39], v8 offset:47168\n\t"
    "buffer_load_dwordx4 v4, s[20:23], s39 offen lds\n\t"
    "v_mfma_f32_16x16x32_bf16 v[68:71], a[172:175], v[48:51], v[68:71]\n\t"
    "ds_read_b64_tr_b16 a[184:185], v16 offset:20488\n\t"
    "v_mfma_f32_16x16x32_bf16 v[72:75], a[152:155], v[40:43], v[72:75]\n\t"
    "ds_read_b64_tr_b16 a[186:187], v16 offset:21256\n\t"
    "ds_read_b128 v[40:43], v8 offset:47232\n\t"
    "v_mfma_f32_16x16x32_bf16 v[72:75], a[164:167], v[44:47], v[72:75]\n\t"
    "ds_read_b64_tr_b16 a[188:189], v17 offset:20480\n\t"
    "v_mfma_f32_16x16x32_bf16 v[72:75], a[176:179], v[48:51], v[72:75]\n\t"
    "ds_read_b64_tr_b16 a[190:191], v17 offset:21248\n\t"
    "s_add_u32 m0, s53, 125952\n\t"
    "s_waitcnt lgkmcnt(11)\n\t"
    "v_mfma_f32_16x16x32_bf16 v[76:79], a[144:147], v[52:55], v[76:79]\n\t"
    "ds_read_b64_tr_b16 a[192:193], v16 offset:26624\n\t"
    "buffer_load_dwordx4 v5, s[20:23], s39 offen lds\n\t"
    "s_waitcnt lgkmcnt(11)\n\t"
    "v_mfma_f32_16x16x32_bf16 v[76:79], a[156:159], v[56:59], v[76:79]\n\t"
    "ds_read_b64_tr_b16 a[194:195], v16 offset:27392\n\t"
    "s_waitcnt lgkmcnt(11)\n\t"
    "v_mfma_f32_16x16x32_bf16 v[76:79], a[168:171], v[60:63], v[76:79]\n\t"
    "ds_read_b64_tr_b16 a[196:197], v16 offset:26632\n\t"
    "v_mfma_f32_16x16x32_bf16 v[80:83], a[148:151], v[52:55], v[80:83]\n\t"
    "ds_read_b64_tr_b16 a[198:199], v16 offset:27400\n\t"
    "ds_read_b128 v[44:47], v9 offset:40960\n\t"
    "v_mfma_f32_16x16x32_bf16 v[80:83], a[160:163], v[56:59], v[80:83]\n\t"
    "ds_read_b64_tr_b16 a[200:201], v17 offset:26624\n\t"
    "s_waitcnt lgkmcnt(14)\n\t"
    "ds_read_b128 v[48:51], v9 offset:41024\n\t"
    "s_add_u32 m0, s53, 126976\n\t"
    "v_mfma_f32_16x16x32_bf16 v[80:83], a[172:175], v[60:63], v[80:83]\n\t"
    "s_waitcnt lgkmcnt(14)\n\t"
    "ds_read_b64_tr_b16 a[202:203], v17 offset:27392\n\t"
    "buffer_load_dwordx4 v6, s[20:23], s39 offen lds\n\t"
    "v_mfma_f32_16x16x32_bf16 v[84:87], a[152:155], v[52:55], v[84:87]\n\t"
    "s_waitcnt lgkmcnt(14)\n\t"
    "ds_read_b64_tr_b16 a[204:205], v16 offset:32768\n\t"
    "s_waitcnt lgkmcnt(14)\n\t"
    "ds_read_b128 v[52:55], v9 offset:41088\n\t"
    "v_mfma_f32_16x16x32_bf16 v[84:87], a[164:167], v[56:59], v[84:87]\n\t"
    "s_waitcnt lgkmcnt(14)\n\t"
    "ds_read_b64_tr_b16 a[206:207], v16 offset:33536\n\t"
    "v_mfma_f32_16x16x32_bf16 v[84:87], a[176:179], v[60:63], v[84:87]\n\t"
    "s_waitcnt lgkmcnt(14)\n\t"
    "ds_read_b64_tr_b16 a[208:209], v16 offset:32776\n\t"
    "v_mfma_f32_16x16x32_bf16 v[88:91], a[144:147], v[32:35], v[88:91]\n\t"
    "s_waitcnt lgkmcnt(14)\n\t"
    "ds_read_b64_tr_b16 a[210:211], v16 offset:33544\n\t"
    "v_mfma_f32_16x16x32_bf16 v[88:91], a[156:159], v[36:39], v[88:91]\n\t"
    "s_waitcnt lgkmcnt(14)\n\t"
    "ds_read_b64_tr_b16 a[212:213], v17 offset:32768\n\t"
    "v_mfma_f32_16x16x32_bf16 v[88:91], a[168:171], v[40:43], v[88:91]\n\t"
    "s_waitcnt lgkmcnt(14)\n\t"
    "ds_read_b64_tr_b16 a[214:215], v17 offset:33536\n\t"
    "v_mfma_f32_16x16x32_bf16 v[92:95], a[148:151], v[32:35], v[92:95]\n\t"
    "s_waitcnt lgkmcnt(14)\n\t"
    "ds_read_b128 v[56:59], v9 offset:44032\n\t"
    "v_mfma_f32_16x16x32_bf16 v[92:95], a[160:163], v[36:39], v[92:95]\n\t"
    "s_waitcnt lgkmcnt(14)\n\t"
    "ds_read_b128 v[60:63], v9 offset:44096\n\t"
    "v_mfma_f32_16x16x32_bf16 v[92:95], a[172:175], v[40:43], v[92:95]\n\t"
    "v_mfma_f32_16x16x32_bf16 v[96:99], a[152:155], v[32:35], v[96:99]\n\t"
    "s_waitcnt lgkmcnt(14)\n\t"
    "ds_read_b128 v[32:35], v9 offset:44160\n\t"
    "v_mfma_f32_16x16x32_bf16 v[96:99], a[164:167], v[36:39], v[96:99]\n\t"
    "v_mfma_f32_16x16x32_bf16 v[96:99], a[176:179], v[40:43], v[96:99]\n\t"
    "s_waitcnt lgkmcnt(13)\n\t"
    "v_mfma_f32_16x16x32_bf16 v[64:67], a[180:183], v[44:47], v[64:67]\n\t"
    "s_waitcnt lgkmcnt(11)\n\t"
    "v_mfma_f32_16x16x32_bf16 v[64:67], a[192:195], v[48:51], v[64:67]\n\t"
    "s_waitcnt lgkmcnt(7)\n\t"
    "v_mfma_f32_16x16x32_bf16 v[64:67], a[204:207], v[52:55], v[64:67]\n\t"
    "s_waitcnt vmcnt(15)\n\t"
    "s_waitcnt lgkmcnt(3)\n\t"
    "s_barrier\n\t"
    "v_mfma_f32_16x16x32_bf16 v[68:71], a[184:187], v[44:47], v[68:71]\n\t"
    "ds_read_b64_tr_b16 a[216:217], v18 offset:0\n\t"
    "ds_read_b128 v[36:39], v9 offset:47104\n\t"
    "v_mfma_f32_16x16x32_bf16 v[68:71], a[196:199], v[48:51], v[68:71]\n\t"
    "ds_read_b64_tr_b16 a[218:219], v18 offset:768\n\t"
    "ds_read_b128 v[40:43], v9 offset:47168\n\t"
    "v_mfma_f32_16x16x32_bf16 v[68:71], a[208:211], v[52:55], v[68:71]\n\t"
    "ds_read_b64_tr_b16 a[220:221], v18 offset:8\n\t"
    "v_mfma_f32_16x16x32_bf16 v[72:75], a[188:191], v[44:47], v[72:75]\n\t"
    "ds_read_b64_tr_b16 a[222:223], v18 offset:776\n\t"
    "ds_read_b128 v[44:47], v9 offset:47232\n\t"
    "v_mfma_f32_16x16x32_bf16 v[72:75], a[200:203], v[48:51], v[72:75]\n\t"
    "ds_read_b64_tr_b16 a[224:225], v19 offset:0\n\t"
    "v_mfma_f32_16x16x32_bf16 v[72:75], a[212:215], v[52:55], v[72:75]\n\t"
    "ds_read_b64_tr_b16 a[226:227], v19 offset:768\n\t"
    "s_waitcnt lgkmcnt(11)\n\t"
    "v_mfma_f32_16x16x32_bf16 v[76:79], a[180:183], v[56:59], v[76:79]\n\t"
    "ds_read_b64_tr_b16 a[228:229], v18 offset:6144\n\t"
    "s_waitcnt lgkmcnt(11)\n\t"
    "v_mfma_f32_16x16x32_bf16 v[76:79], a[192:195], v[60:63], v[76:79]\n\t"
    "ds_read_b64_tr_b16 a[230:231], v18 offset:6912\n\t"
    "s_waitcnt lgkmcnt(11)\n\t"
    "v_mfma_f32_16x16x32_bf16 v[76:79], a[204:207], v[32:35], v[76:79]\n\t"
    "ds_read_b64_tr_b16 a[232:233], v18 offset:6152\n\t"
    "v_mfma_f32_16x16x32_bf16 v[80:83], a[184:187], v[56:59], v[80:83]\n\t"
    "ds_read_b64_tr_b16 a[234:235], v18 offset:6920\n\t"
    "ds_read_b128 v[48:51], v10 offset:20480\n\t"
    "v_mfma_f32_16x16x32_bf16 v[80:83], a[196:199], v[60:63], v[80:83]\n\t"
    "ds_read_b64_tr_b16 a[236:237], v19 offset:6144\n\t"
    "s_waitcnt lgkmcnt(14)\n\t"
    "ds_read_b128 v[52:55], v10 offset:20544\n\t"
    "v_mfma_f32_16x16x32_bf16 v[80:83], a[208:211], v[32:35], v[80:83]\n\t"
    "s_waitcnt lgkmcnt(14)\n\t"
    "ds_read_b64_tr_b16 a[238:239], v19 offset:6912\n\t"
    "v_mfma_f32_16x16x32_bf16 v[84:87], a[188:191], v[56:59], v[84:87]\n\t"
    "s_waitcnt lgkmcnt(14)\n\t"
    "ds_read_b64_tr_b16 a[240:241], v18 offset:12288\n\t"
    "s_waitcnt lgkmcnt(14)\n\t"
    "ds_read_b128 v[56:59], v10 offset:20608\n\t"
    "v_mfma_f32_16x16x32_bf16 v[84:87], a[200:203], v[60:63], v[84:87]\n\t"
    "s_waitcnt lgkmcnt(14)\n\t"
    "ds_read_b64_tr_b16 a[242:243], v18 offset:13056\n\t"
    "v_mfma_f32_16x16x32_bf16 v[84:87], a[212:215], v[32:35], v[84:87]\n\t"
    "s_waitcnt lgkmcnt(14)\n\t"
    "ds_read_b64_tr_b16 a[244:245], v18 offset:12296\n\t"
    "v_mfma_f32_16x16x32_bf16 v[88:91], a[180:183], v[36:39], v[88:91]\n\t"
    "s_waitcnt lgkmcnt(14)\n\t"
    "ds_read_b64_tr_b16 a[246:247], v18 offset:13064\n\t"
    "v_mfma_f32_16x16x32_bf16 v[88:91], a[192:195], v[40:43], v[88:91]\n\t"
    "s_waitcnt lgkmcnt(14)\n\t"
    "ds_read_b64_tr_b16 a[248:249], v19 offset:12288\n\t"
    "v_mfma_f32_16x16x32_bf16 v[88:91], a[204:207], v[44:47], v[88:91]\n\t"
    "s_waitcnt lgkmcnt(14)\n\t"
    "ds_read_b64_tr_b16 a[250:251], v19 offset:13056\n\t"
    "v_mfma_f32_16x16x32_bf16 v[92:95], a[184:187], v[36:39], v[92:95]\n\t"
    "v_mfma_f32_16x16x32_bf16 v[92:95], a[196:199], v[40:43], v[92:95]\n\t"
    "v_mfma_f32_16x16x32_bf16 v[92:95], a[208:211], v[44:47], v[92:95]\n\t"
    "v_mfma_f32_16x16x32_bf16 v[96:99], a[188:191], v[36:39], v[96:99]\n\t"
    "v_mfma_f32_16x16x32_bf16 v[96:99], a[200:203], v[40:43], v[96:99]\n\t"
    "v_mfma_f32_16x16x32_bf16 v[96:99], a[212:215], v[44:47], v[96:99]\n\t"
    "s_waitcnt lgkmcnt(10)\n\t"
    "v_mfma_f32_16x16x32_bf16 v[64:67], a[216:219], v[48:51], v[64:67]\n\t"
    "s_waitcnt lgkmcnt(8)\n\t"
    "v_mfma_f32_16x16x32_bf16 v[64:67], a[228:231], v[52:55], v[64:67]\n\t"
    "s_waitcnt lgkmcnt(4)\n\t"
    "v_mfma_f32_16x16x32_bf16 v[64:67], a[240:243], v[56:59], v[64:67]\n\t"
    "v_mfma_f32_16x16x32_bf16 v[68:71], a[220:223], v[48:51], v[68:71]\n\t"
    "ds_read_b128 v[60:63], v10 offset:23552\n\t"
    "v_mfma_f32_16x16x32_bf16 v[68:71], a[232:235], v[52:55], v[68:71]\n\t"
    "ds_read_b128 v[32:35], v10 offset:23616\n\t"
    "s_add_u32 m0, s53, 143360\n\t"
    "s_waitcnt lgkmcnt(4)\n\t"
    "v_mfma_f32_16x16x32_bf16 v[68:71], a[244:247], v[56:59], v[68:71]\n\t"
    "ds_read_b128 v[36:39], v10 offset:23680\n\t"
    "buffer_load_dwordx4 v2, s[20:23], s40 offen lds\n\t"
    "v_mfma_f32_16x16x32_bf16 v[72:75], a[224:227], v[48:51], v[72:75]\n\t"
    "v_mfma_f32_16x16x32_bf16 v[72:75], a[236:239], v[52:55], v[72:75]\n\t"
    "s_waitcnt lgkmcnt(3)\n\t"
    "v_mfma_f32_16x16x32_bf16 v[72:75], a[248:251], v[56:59], v[72:75]\n\t"
    "v_mfma_f32_16x16x32_bf16 v[100:103], a[108:111], v[48:51], v[100:103]\n\t"
    "v_mfma_f32_16x16x32_bf16 v[100:103], a[120:123], v[52:55], v[100:103]\n\t"
    "v_mfma_f32_16x16x32_bf16 v[100:103], a[132:135], v[56:59], v[100:103]\n\t"
    "v_mfma_f32_16x16x32_bf16 v[104:107], a[112:115], v[48:51], v[104:107]\n\t"
    "v_mfma_f32_16x16x32_bf16 v[104:107], a[124:127], v[52:55], v[104:107]\n\t"
    "v_mfma_f32_16x16x32_bf16 v[104:107], a[136:139], v[56:59], v[104:107]\n\t"
    "v_mfma_f32_16x16x32_bf16 v[108:111], a[116:119], v[48:51], v[108:111]\n\t"
    "v_mfma_f32_16x16x32_bf16 v[108:111], a[128:131], v[52:55], v[108:111]\n\t"
    "v_mfma_f32_16x16x32_bf16 v[108:111], a[140:143], v[56:59], v[108:111]\n\t"
    "s_waitcnt lgkmcnt(2)\n\t"
    "v_mfma_f32_16x16x32_bf16 v[76:79], a[216:219], v[60:63], v[76:79]\n\t"
    "s_waitcnt lgkmcnt(1)\n\t"
    "v_mfma_f32_16x16x32_bf16 v[76:79], a[228:231], v[32:35], v[76:79]\n\t"
    "s_waitcnt lgkmcnt(0)\n\t"
    "v_mfma_f32_16x16x32_bf16 v[76:79], a[240:243], v[36:39], v[76:79]\n\t"
    "v_mfma_f32_16x16x32_bf16 v[80:83], a[220:223], v[60:63], v[80:83]\n\t"
    "ds_read_b128 v[40:43], v10 offset:26624\n\t"
    "v_mfma_f32_16x16x32_bf16 v[80:83], a[232:235], v[32:35], v[80:83]\n\t"
    "ds_read_b128 v[44:47], v10 offset:26688\n\t"
    "v_mfma_f32_16x16x32_bf16 v[80:83], a[244:247], v[36:39], v[80:83]\n\t"
    "ds_read_b128 v[48:51], v10 offset:26752\n\t"
    "v_mfma_f32_16x16x32_bf16 v[84:87], a[224:227], v[60:63], v[84:87]\n\t"
    "v_mfma_f32_16x16x32_bf16 v[84:87], a[236:239], v[32:35], v[84:87]\n\t"
    "v_mfma_f32_16x16x32_bf16 v[84:87], a[248:251], v[36:39], v[84:87]\n\t"
    "v_mfma_f32_16x16x32_bf16 v[112:115], a[108:111], v[60:63], v[112:115]\n\t"
    "v_mfma_f32_16x16x32_bf16 v[112:115], a[120:123], v[32:35], v[112:115]\n\t"
    "v_mfma_f32_16x16x32_bf16 v[112:115], a[132:135], v[36:39], v[112:115]\n\t"
    "s_waitcnt vmcnt(11)\n\t"
    "s_barrier\n\t"
    "v_mfma_f32_16x16x32_bf16 v[116:119], a[112:115], v[60:63], v[116:119]\n\t"
    "ds_read_b64_tr_b16 a[252:253], v16 offset:0\n\t"
    "v_mfma_f32_16x16x32_bf16 v[116:119], a[124:127], v[32:35], v[116:119]\n\t"
    "ds_read_b64_tr_b16 a[254:255], v16 offset:768\n\t"
    "v_mfma_f32_16x16x32_bf16 v[116:119], a[136:139], v[36:39], v[116:119]\n\t"
    "ds_read_b64_tr_b16 v[188:189], v16 offset:8\n\t"
    "v_mfma_f32_16x16x32_bf16 v[120:123], a[116:119], v[60:63], v[120:123]\n\t"
    "ds_read_b64_tr_b16 v[190:191], v16 offset:776\n\t"
    "v_mfma_f32_16x16x32_bf16 v[120:123], a[128:131], v[32:35], v[120:123]\n\t"
    "ds_read_b64_tr_b16 v[192:193], v17 offset:0\n\t"
    "v_mfma_f32_16x16x32_bf16 v[120:123], a[140:143], v[36:39], v[120:123]\n\t"
    "ds_read_b64_tr_b16 v[194:195], v17 offset:768\n\t"
    "s_waitcnt lgkmcnt(8)\n\t"
    "v_mfma_f32_16x16x32_bf16 v[88:91], a[216:219], v[40:43], v[88:91]\n\t"
    "ds_read_b64_tr_b16 v[196:197], v16 offset:6144\n\t"
    "s_waitcnt lgkmcnt(8)\n\t"
    "v_mfma_f32_16x16x32_bf16 v[88:91], a[228:231], v[44:47], v[88:91]\n\t"
    "ds_read_b64_tr_b16 v[198:199], v16 offset:6912\n\t"
    "s_waitcnt lgkmcnt(8)\n\t"
    "v_mfma_f32_16x16x32_bf16 v[88:91], a[240:243], v[48:51], v[88:91]\n\t"
    "ds_read_b64_tr_b16 v[200:201], v16 offset:6152\n\t"
    "v_mfma_f32_16x16x32_bf16 v[92:95], a[220:223], v[40:43], v[92:95]\n\t"
    "ds_read_b64_tr_b16 v[202:203], v16 offset:6920\n\t"
    "ds_read_b128 v[52:55], v11 offset:20480\n\t"
    "v_mfma_f32_16x16x32_bf16 v[92:95], a[232:235], v[44:47], v[92:95]\n\t"
    "ds_read_b64_tr_b16 v[204:205], v17 offset:6144\n\t"
    "ds_read_b128 v[56:59], v11 offset:20544\n\t"
    "v_mfma_f32_16x16x32_bf16 v[92:95], a[244:247], v[48:51], v[92:95]\n\t"
    "ds_read_b64_tr_b16 v[206:207], v17 offset:6912\n\t"
    "ds_read_b128 v[60:63], v11 offset:20608\n\t"
    "v_mfma_f32_16x16x32_bf16 v[96:99], a[224:227], v[40:43], v[96:99]\n\t"
    "s_waitcnt lgkmcnt(14)\n\t"
    "ds_read_b64_tr_b16 v[208:209], v16 offset:12288\n\t"
    "v_mfma_f32_16x16x32_bf16 v[96:99], a[236:239], v[44:47], v[96:99]\n\t"
    "s_waitcnt lgkmcnt(14)\n\t"
    "ds_read_b64_tr_b16 v[210:211], v16 offset:13056\n\t"
    "v_mfma_f32_16x16x32_bf16 v[96:99], a[248:251], v[48:51], v[96:99]\n\t"
    "s_waitcnt lgkmcnt(14)\n\t"
    "ds_read_b64_tr_b16 v[212:213], v16 offset:12296\n\t"
    "s_add_u32 m0, s53, 144384\n\t"
    "v_mfma_f32_16x16x32_bf16 v[124:127], a[108:111], v[40:43], v[124:127]\n\t"
    "s_waitcnt lgkmcnt(14)\n\t"
    "ds_read_b64_tr_b16 v[214:215], v16 offset:13064\n\t"
    "buffer_load_dwordx4 v3, s[20:23], s40 offen lds\n\t"
    "v_mfma_f32_16x16x32_bf16 v[124:127], a[120:123], v[44:47], v[124:127]\n\t"
    "s_waitcnt lgkmcnt(14)\n\t"
    "ds_read_b64_tr_b16 v[216:217], v17 offset:12288\n\t"
    "v_mfma_f32_16x16x32_bf16 v[124:127], a[132:135], v[48:51], v[124:127]\n\t"
    "s_waitcnt lgkmcnt(14)\n\t"
    "ds_read_b64_tr_b16 v[218:219], v17 offset:13056\n\t"
    "v_mfma_f32_16x16x32_bf16 v[128:131], a[112:115], v[40:43], v[128:131]\n\t"
    "v_mfma_f32_16x16x32_bf16 v[128:131], a[124:127], v[44:47], v[128:131]\n\t"
    "v_mfma_f32_16x16x32_bf16 v[128:131], a[136:139], v[48:51], v[128:131]\n\t"
    "v_mfma_f32_16x16x32_bf16 v[132:135], a[116:119], v[40:43], v[132:135]\n\t"
    "v_mfma_f32_16x16x32_bf16 v[132:135], a[128:131], v[44:47], v[132:135]\n\t"
    "v_mfma_f32_16x16x32_bf16 v[132:135], a[140:143], v[48:51], v[132:135]\n\t"
    "s_waitcnt lgkmcnt(10)\n\t"
    "v_mfma_f32_16x16x32_bf16 v[64:67], a[252:255], v[52:55], v[64:67]\n\t"
    "s_waitcnt lgkmcnt(8)\n\t"
    "v_mfma_f32_16x16x32_bf16 v[64:67], v[196:199], v[56:59], v[64:67]\n\t"
    "s_waitcnt lgkmcnt(4)\n\t"
    "v_mfma_f32_16x16x32_bf16 v[64:67], v[208:211], v[60:63], v[64:67]\n\t"
    "v_mfma_f32_16x16x32_bf16 v[68:71], v[188:191], v[52:55], v[68:71]\n\t"
    "ds_read_b128 v[32:35], v11 offset:23552\n\t"
    "v_mfma_f32_16x16x32_bf16 v[68:71], v[200:203], v[56:59], v[68:71]\n\t"
    "ds_read_b128 v[36:39], v11 offset:23616\n\t"
    "s_waitcnt lgkmcnt(4)\n\t"
    "v_mfma_f32_16x16x32_bf16 v[68:71], v[212:215], v[60:63], v[68:71]\n\t"
    "ds_read_b128 v[40:43], v11 offset:23680\n\t"
    "v_mfma_f32_16x16x32_bf16 v[72:75], v[192:195], v[52:55], v[72:75]\n\t"
    "v_mfma_f32_16x16x32_bf16 v[72:75], v[204:207], v[56:59], v[72:75]\n\t"
    "s_waitcnt lgkmcnt(3)\n\t"
    "v_mfma_f32_16x16x32_bf16 v[72:75], v[216:219], v[60:63], v[72:75]\n\t"
    "v_mfma_f32_16x16x32_bf16 v[100:103], a[144:147], v[52:55], v[100:103]\n\t"
    "v_mfma_f32_16x16x32_bf16 v[100:103], a[156:159], v[56:59], v[100:103]\n\t"
    "v_mfma_f32_16x16x32_bf16 v[100:103], a[168:171], v[60:63], v[100:103]\n\t"
    "v_mfma_f32_16x16x32_bf16 v[104:107], a[148:151], v[52:55], v[104:107]\n\t"
    "v_mfma_f32_16x16x32_bf16 v[104:107], a[160:163], v[56:59], v[104:107]\n\t"
    "v_mfma_f32_16x16x32_bf16 v[104:107], a[172:175], v[60:63], v[104:107]\n\t"
    "v_mfma_f32_16x16x32_bf16 v[108:111], a[152:155], v[52:55], v[108:111]\n\t"
    "v_mfma_f32_16x16x32_bf16 v[108:111], a[164:167], v[56:59], v[108:111]\n\t"
    "v_mfma_f32_16x16x32_bf16 v[108:111], a[176:179], v[60:63], v[108:111]\n\t"
    "s_waitcnt lgkmcnt(2)\n\t"
    "v_mfma_f32_16x16x32_bf16 v[76:79], a[252:255], v[32:35], v[76:79]\n\t"
    "s_waitcnt lgkmcnt(1)\n\t"
    "v_mfma_f32_16x16x32_bf16 v[76:79], v[196:199], v[36:39], v[76:79]\n\t"
    "s_waitcnt lgkmcnt(0)\n\t"
    "v_mfma_f32_16x16x32_bf16 v[76:79], v[208:211], v[40:43], v[76:79]\n\t"
    "v_mfma_f32_16x16x32_bf16 v[80:83], v[188:191], v[32:35], v[80:83]\n\t"
    "ds_read_b128 v[44:47], v11 offset:26624\n\t"
    "v_mfma_f32_16x16x32_bf16 v[80:83], v[200:203], v[36:39], v[80:83]\n\t"
    "ds_read_b128 v[48:51], v11 offset:26688\n\t"
    "v_mfma_f32_16x16x32_bf16 v[80:83], v[212:215], v[40:43], v[80:83]\n\t"
    "ds_read_b128 v[52:55], v11 offset:26752\n\t"
    "v_mfma_f32_16x16x32_bf16 v[84:87], v[192:195], v[32:35], v[84:87]\n\t"
    "v_mfma_f32_16x16x32_bf16 v[84:87], v[204:207], v[36:39], v[84:87]\n\t"
    "v_mfma_f32_16x16x32_bf16 v[84:87], v[216:219], v[40:43], v[84:87]\n\t"
    "v_mfma_f32_16x16x32_bf16 v[112:115], a[144:147], v[32:35], v[112:115]\n\t"
    "v_mfma_f32_16x16x32_bf16 v[112:115], a[156:159], v[36:39], v[112:115]\n\t"
    "v_mfma_f32_16x16x32_bf16 v[112:115], a[168:171], v[40:43], v[112:115]\n\t"
    "s_waitcnt vmcnt(7)\n\t"
    "s_barrier\n\t"
    "v_mfma_f32_16x16x32_bf16 v[116:119], a[148:151], v[32:35], v[116:119]\n\t"
    "ds_read_b64_tr_b16 v[220:221], v18 offset:40960\n\t"
    "s_add_u32 m0, s53, 145408\n\t"
    "v_mfma_f32_16x16x32_bf16 v[116:119], a[160:163], v[36:39], v[116:119]\n\t"
    "ds_read_b64_tr_b16 v[222:223], v18 offset:41728\n\t"
    "buffer_load_dwordx4 v4, s[20:23], s40 offen lds\n\t"
    "v_mfma_f32_16x16x32_bf16 v[116:119], a[172:175], v[40:43], v[116:119]\n\t"
    "ds_read_b64_tr_b16 v[224:225], v18 offset:40968\n\t"
    "v_mfma_f32_16x16x32_bf16 v[120:123], a[152:155], v[32:35], v[120:123]\n\t"
    "ds_read_b64_tr_b16 v[226:227], v18 offset:41736\n\t"
    "v_mfma_f32_16x16x32_bf16 v[120:123], a[164:167], v[36:39], v[120:123]\n\t"
    "ds_read_b64_tr_b16 v[228:229], v19 offset:40960\n\t"
    "v_mfma_f32_16x16x32_bf16 v[120:123], a[176:179], v[40:43], v[120:123]\n\t"
    "ds_read_b64_tr_b16 v[230:231], v19 offset:41728\n\t"
    "s_waitcnt lgkmcnt(8)\n\t"
    "v_mfma_f32_16x16x32_bf16 v[88:91], a[252:255], v[44:47], v[88:91]\n\t"
    "ds_read_b64_tr_b16 v[232:233], v18 offset:47104\n\t"
    "s_waitcnt lgkmcnt(8)\n\t"
    "v_mfma_f32_16x16x32_bf16 v[88:91], v[196:199], v[48:51], v[88:91]\n\t"
    "ds_read_b64_tr_b16 v[234:235], v18 offset:47872\n\t"
    "s_waitcnt lgkmcnt(8)\n\t"
    "v_mfma_f32_16x16x32_bf16 v[88:91], v[208:211], v[52:55], v[88:91]\n\t"
    "ds_read_b64_tr_b16 v[236:237], v18 offset:47112\n\t"
    "v_mfma_f32_16x16x32_bf16 v[92:95], v[188:191], v[44:47], v[92:95]\n\t"
    "ds_read_b64_tr_b16 v[238:239], v18 offset:47880\n\t"
    "ds_read_b128 v[56:59], v12 offset:20480\n\t"
    "v_mfma_f32_16x16x32_bf16 v[92:95], v[200:203], v[48:51], v[92:95]\n\t"
    "ds_read_b64_tr_b16 v[240:241], v19 offset:47104\n\t"
    "ds_read_b128 v[60:63], v12 offset:20544\n\t"
    "v_mfma_f32_16x16x32_bf16 v[92:95], v[212:215], v[52:55], v[92:95]\n\t"
    "ds_read_b64_tr_b16 v[242:243], v19 offset:47872\n\t"
    "ds_read_b128 v[32:35], v12 offset:20608\n\t"
    "v_mfma_f32_16x16x32_bf16 v[96:99], v[192:195], v[44:47], v[96:99]\n\t"
    "s_waitcnt lgkmcnt(14)\n\t"
    "ds_read_b64_tr_b16 v[244:245], v18 offset:53248\n\t"
    "v_mfma_f32_16x16x32_bf16 v[96:99], v[204:207], v[48:51], v[96:99]\n\t"
    "s_waitcnt lgkmcnt(14)\n\t"
    "ds_read_b64_tr_b16 v[246:247], v18 offset:54016\n\t"
    "v_mfma_f32_16x16x32_bf16 v[96:99], v[216:219], v[52:55], v[96:99]\n\t"
    "s_waitcnt lgkmcnt(14)\n\t"
    "ds_read_b64_tr_b16 v[248:249], v18 offset:53256\n\t"
    "v_mfma_f32_16x16x32_bf16 v[124:127], a[144:147], v[44:47], v[124:127]\n\t"
    "s_waitcnt lgkmcnt(14)\n\t"
    "ds_read_b64_tr_b16 v[250:251], v18 offset:54024\n\t"
    "v_mfma_f32_16x16x32_bf16 v[124:127], a[156:159], v[48:51], v[124:127]\n\t"
    "s_waitcnt lgkmcnt(14)\n\t"
    "ds_read_b64_tr_b16 v[252:253], v19 offset:53248\n\t"
    "v_mfma_f32_16x16x32_bf16 v[124:127], a[168:171], v[52:55], v[124:127]\n\t"
    "s_waitcnt lgkmcnt(14)\n\t"
    "ds_read_b64_tr_b16 v[254:255], v19 offset:54016\n\t"
    "v_mfma_f32_16x16x32_bf16 v[128:131], a[148:151], v[44:47], v[128:131]\n\t"
    "v_mfma_f32_16x16x32_bf16 v[128:131], a[160:163], v[48:51], v[128:131]\n\t"
    "v_mfma_f32_16x16x32_bf16 v[128:131], a[172:175], v[52:55], v[128:131]\n\t"
    "v_mfma_f32_16x16x32_bf16 v[132:135], a[152:155], v[44:47], v[132:135]\n\t"
    "v_mfma_f32_16x16x32_bf16 v[132:135], a[164:167], v[48:51], v[132:135]\n\t"
    "v_mfma_f32_16x16x32_bf16 v[132:135], a[176:179], v[52:55], v[132:135]\n\t"
    "s_waitcnt lgkmcnt(10)\n\t"
    "v_mfma_f32_16x16x32_bf16 v[64:67], v[220:223], v[56:59], v[64:67]\n\t"
    "s_waitcnt lgkmcnt(8)\n\t"
    "v_mfma_f32_16x16x32_bf16 v[64:67], v[232:235], v[60:63], v[64:67]\n\t"
    "s_waitcnt lgkmcnt(4)\n\t"
    "v_mfma_f32_16x16x32_bf16 v[64:67], v[244:247], v[32:35], v[64:67]\n\t"
    "v_mfma_f32_16x16x32_bf16 v[68:71], v[224:227], v[56:59], v[68:71]\n\t"
    "ds_read_b128 v[36:39], v12 offset:23552\n\t"
    "v_mfma_f32_16x16x32_bf16 v[68:71], v[236:239], v[60:63], v[68:71]\n\t"
    "ds_read_b128 v[40:43], v12 offset:23616\n\t"
    "s_waitcnt lgkmcnt(4)\n\t"
    "v_mfma_f32_16x16x32_bf16 v[68:71], v[248:251], v[32:35], v[68:71]\n\t"
    "ds_read_b128 v[44:47], v12 offset:23680\n\t"
    "v_mfma_f32_16x16x32_bf16 v[72:75], v[228:231], v[56:59], v[72:75]\n\t"
    "v_mfma_f32_16x16x32_bf16 v[72:75], v[240:243], v[60:63], v[72:75]\n\t"
    "s_waitcnt lgkmcnt(3)\n\t"
    "v_mfma_f32_16x16x32_bf16 v[72:75], v[252:255], v[32:35], v[72:75]\n\t"
    "v_mfma_f32_16x16x32_bf16 v[100:103], a[180:183], v[56:59], v[100:103]\n\t"
    "v_mfma_f32_16x16x32_bf16 v[100:103], a[192:195], v[60:63], v[100:103]\n\t"
    "v_mfma_f32_16x16x32_bf16 v[100:103], a[204:207], v[32:35], v[100:103]\n\t"
    "v_mfma_f32_16x16x32_bf16 v[104:107], a[184:187], v[56:59], v[104:107]\n\t"
    "v_mfma_f32_16x16x32_bf16 v[104:107], a[196:199], v[60:63], v[104:107]\n\t"
    "v_mfma_f32_16x16x32_bf16 v[104:107], a[208:211], v[32:35], v[104:107]\n\t"
    "v_mfma_f32_16x16x32_bf16 v[108:111], a[188:191], v[56:59], v[108:111]\n\t"
    "v_mfma_f32_16x16x32_bf16 v[108:111], a[200:203], v[60:63], v[108:111]\n\t"
    "s_add_u32 s77, s44, 0\n\t"
    "s_add_u32 m0, s53, 146432\n\t"
    "v_mfma_f32_16x16x32_bf16 v[108:111], a[212:215], v[32:35], v[108:111]\n\t"
    "v_cvt_pk_bf16_f32 v176, v72, v73\n\t"
    "buffer_load_dwordx4 v5, s[20:23], s40 offen lds\n\t"
    "s_waitcnt lgkmcnt(2)\n\t"
    "v_mfma_f32_16x16x32_bf16 v[76:79], v[220:223], v[36:39], v[76:79]\n\t"
    "v_cvt_pk_bf16_f32 v177, v74, v75\n\t"
    "s_waitcnt lgkmcnt(1)\n\t"
    "v_mfma_f32_16x16x32_bf16 v[76:79], v[232:235], v[40:43], v[76:79]\n\t"
    "ds_write_b64 v25, v[176:177] offset:0\n\t"
    "s_waitcnt lgkmcnt(1)\n\t"
    "v_mfma_f32_16x16x32_bf16 v[76:79], v[244:247], v[44:47], v[76:79]\n\t"
    "v_cvt_pk_bf16_f32 v172, v64, v65\n\t"
    "v_mfma_f32_16x16x32_bf16 v[80:83], v[224:227], v[36:39], v[80:83]\n\t"
    "v_cvt_pk_bf16_f32 v173, v66, v67\n\t"
    "ds_read_b128 v[48:51], v12 offset:26624\n\t"
    "v_mfma_f32_16x16x32_bf16 v[80:83], v[236:239], v[40:43], v[80:83]\n\t"
    "v_cvt_pk_bf16_f32 v174, v68, v69\n\t"
    "ds_read_b128 v[52:55], v12 offset:26688\n\t"
    "v_mfma_f32_16x16x32_bf16 v[80:83], v[248:251], v[44:47], v[80:83]\n\t"
    "v_cvt_pk_bf16_f32 v175, v70, v71\n\t"
    "ds_read_b128 v[56:59], v12 offset:26752\n\t"
    "v_mfma_f32_16x16x32_bf16 v[84:87], v[228:231], v[36:39], v[84:87]\n\t"
    "buffer_store_dwordx4 v[172:175], v22, s[28:31], s77 offen nt\n\t"
    "v_mfma_f32_16x16x32_bf16 v[84:87], v[240:243], v[40:43], v[84:87]\n\t"
    "v_mfma_f32_16x16x32_bf16 v[84:87], v[252:255], v[44:47], v[84:87]\n\t"
    "v_mfma_f32_16x16x32_bf16 v[112:115], a[180:183], v[36:39], v[112:115]\n\t"
    "v_mfma_f32_16x16x32_bf16 v[112:115], a[192:195], v[40:43], v[112:115]\n\t"
    "v_mfma_f32_16x16x32_bf16 v[112:115], a[204:207], v[44:47], v[112:115]\n\t"
    "v_mfma_f32_16x16x32_bf16 v[116:119], a[184:187], v[36:39], v[116:119]\n\t"
    "v_mfma_f32_16x16x32_bf16 v[116:119], a[196:199], v[40:43], v[116:119]\n\t"
    "v_mfma_f32_16x16x32_bf16 v[116:119], a[208:211], v[44:47], v[116:119]\n\t"
    "v_mfma_f32_16x16x32_bf16 v[120:123], a[188:191], v[36:39], v[120:123]\n\t"
    "v_mfma_f32_16x16x32_bf16 v[120:123], a[200:203], v[40:43], v[120:123]\n\t"
    "s_add_u32 s78, s44, 3072\n\t"
    "v_mfma_f32_16x16x32_bf16 v[120:123], a[212:215], v[44:47], v[120:123]\n\t"
    "v_cvt_pk_bf16_f32 v182, v84, v85\n\t"
    "s_waitcnt lgkmcnt(2)\n\t"
    "v_mfma_f32_16x16x32_bf16 v[88:91], v[220:223], v[48:51], v[88:91]\n\t"
    "v_cvt_pk_bf16_f32 v183, v86, v87\n\t"
    "s_waitcnt lgkmcnt(1)\n\t"
    "v_mfma_f32_16x16x32_bf16 v[88:91], v[232:235], v[52:55], v[88:91]\n\t"
    "s_waitcnt vmcnt(5)\n\t"
    "s_waitcnt lgkmcnt(0)\n\t"
    "s_barrier\n\t"
    "ds_write_b64 v25, v[182:183] offset:1280\n\t"
    "v_mfma_f32_16x16x32_bf16 v[88:91], v[244:247], v[56:59], v[88:91]\n\t"
    "v_cvt_pk_bf16_f32 v178, v76, v77\n\t"
    "v_mfma_f32_16x16x32_bf16 v[92:95], v[224:227], v[48:51], v[92:95]\n\t"
    "v_cvt_pk_bf16_f32 v179, v78, v79\n\t"
    "ds_read_b128 v[60:63], v13 offset:0\n\t"
    "v_mfma_f32_16x16x32_bf16 v[92:95], v[236:239], v[52:55], v[92:95]\n\t"
    "v_cvt_pk_bf16_f32 v180, v80, v81\n\t"
    "ds_read_b128 v[32:35], v13 offset:64\n\t"
    "v_mfma_f32_16x16x32_bf16 v[92:95], v[248:251], v[56:59], v[92:95]\n\t"
    "v_cvt_pk_bf16_f32 v181, v82, v83\n\t"
    "ds_read_b128 v[36:39], v13 offset:128\n\t"
    "v_mfma_f32_16x16x32_bf16 v[96:99], v[228:231], v[48:51], v[96:99]\n\t"
    "buffer_store_dwordx4 v[178:181], v22, s[28:31], s78 offen nt\n\t"
    "v_mfma_f32_16x16x32_bf16 v[96:99], v[240:243], v[52:55], v[96:99]\n\t"
    "v_mfma_f32_16x16x32_bf16 v[96:99], v[252:255], v[56:59], v[96:99]\n\t"
    "v_mfma_f32_16x16x32_bf16 v[124:127], a[180:183], v[48:51], v[124:127]\n\t"
    "v_mfma_f32_16x16x32_bf16 v[124:127], a[192:195], v[52:55], v[124:127]\n\t"
    "s_waitcnt lgkmcnt(3)\n\t"
    "s_barrier\n\t"
    "v_mfma_f32_16x16x32_bf16 v[124:127], a[204:207], v[56:59], v[124:127]\n\t"
    "ds_read_b128 v[28:31], v26 offset:0\n\t"
    "v_mfma_f32_16x16x32_bf16 v[128:131], a[184:187], v[48:51], v[128:131]\n\t"
    "s_add_u32 s96, s44, 0\n\t"
    "s_add_u32 s97, s44, 3072\n\t"
    "v_mfma_f32_16x16x32_bf16 v[128:131], a[196:199], v[52:55], v[128:131]\n\t"
    "s_cmp_eq_u32 s98, 0\n\t"
    "s_cselect_b32 s96, s96, s97\n\t"
    "v_mfma_f32_16x16x32_bf16 v[128:131], a[208:211], v[56:59], v[128:131]\n\t"
    "v_mfma_f32_16x16x32_bf16 v[132:135], a[188:191], v[48:51], v[132:135]\n\t"
    "v_mfma_f32_16x16x32_bf16 v[132:135], a[200:203], v[52:55], v[132:135]\n\t"
    "s_add_u32 s77, s44, 6144\n\t"
    "v_mfma_f32_16x16x32_bf16 v[132:135], a[212:215], v[56:59], v[132:135]\n\t"
    "v_cvt_pk_bf16_f32 v176, v96, v97\n\t"
    "s_waitcnt lgkmcnt(3)\n\t"
    "v_mfma_f32_16x16x32_bf16 v[100:103], a[216:219], v[60:63], v[100:103]\n\t"
    "v_cvt_pk_bf16_f32 v177, v98, v99\n\t"
    "s_waitcnt lgkmcnt(2)\n\t"
    "v_mfma_f32_16x16x32_bf16 v[100:103], a[228:231], v[32:35], v[100:103]\n\t"
    "ds_write_b64 v25, v[176:177] offset:2560\n\t"
    "s_waitcnt lgkmcnt(2)\n\t"
    "v_mfma_f32_16x16x32_bf16 v[100:103], a[240:243], v[36:39], v[100:103]\n\t"
    "v_cvt_pk_bf16_f32 v172, v88, v89\n\t"
    "s_add_u32 m0, s53, 147456\n\t"
    "v_mfma_f32_16x16x32_bf16 v[104:107], a[220:223], v[60:63], v[104:107]\n\t"
    "v_cvt_pk_bf16_f32 v173, v90, v91\n\t"
    "buffer_load_dwordx4 v6, s[20:23], s40 offen lds\n\t"
    "v_mfma_f32_16x16x32_bf16 v[104:107], a[232:235], v[32:35], v[104:107]\n\t"
    "v_cvt_pk_bf16_f32 v174, v92, v93\n\t"
    "v_mfma_f32_16x16x32_bf16 v[104:107], a[244:247], v[36:39], v[104:107]\n\t"
    "s_waitcnt lgkmcnt(1)\n\t"
    "buffer_store_dwordx4 v[28:31], v23, s[28:31], s96 offen nt\n\t"
    "v_cvt_pk_bf16_f32 v175, v94, v95\n\t"
    "v_mfma_f32_16x16x32_bf16 v[108:111], a[224:227], v[60:63], v[108:111]\n\t"
    "buffer_store_dwordx4 v[172:175], v22, s[28:31], s77 offen nt\n\t"
    "v_mfma_f32_16x16x32_bf16 v[108:111], a[236:239], v[32:35], v[108:111]\n\t"
    "v_mfma_f32_16x16x32_bf16 v[108:111], a[248:251], v[36:39], v[108:111]\n\t"
    "v_mfma_f32_16x16x32_bf16 v[136:139], a[108:111], v[60:63], v[136:139]\n\t"
    "v_mfma_f32_16x16x32_bf16 v[136:139], a[120:123], v[32:35], v[136:139]\n\t"
    "v_mfma_f32_16x16x32_bf16 v[136:139], a[132:135], v[36:39], v[136:139]\n\t"
    "v_mfma_f32_16x16x32_bf16 v[140:143], a[112:115], v[60:63], v[140:143]\n\t"
    "ds_read_b128 v[40:43], v14 offset:0\n\t"
    "v_mfma_f32_16x16x32_bf16 v[140:143], a[124:127], v[32:35], v[140:143]\n\t"
    "ds_read_b128 v[44:47], v14 offset:64\n\t"
    "v_mfma_f32_16x16x32_bf16 v[140:143], a[136:139], v[36:39], v[140:143]\n\t"
    "ds_read_b128 v[48:51], v14 offset:128\n\t"
    "v_mfma_f32_16x16x32_bf16 v[144:147], a[116:119], v[60:63], v[144:147]\n\t"
    "v_mfma_f32_16x16x32_bf16 v[144:147], a[128:131], v[32:35], v[144:147]\n\t"
    "v_mfma_f32_16x16x32_bf16 v[144:147], a[140:143], v[36:39], v[144:147]\n\t"
    "v_mfma_f32_16x16x32_bf16 v[64:67], a[0:3], v[60:63], 0\n\t"
    "v_mfma_f32_16x16x32_bf16 v[64:67], a[12:15], v[32:35], v[64:67]\n\t"
    "v_mfma_f32_16x16x32_bf16 v[64:67], a[24:27], v[36:39], v[64:67]\n\t"
    "v_mfma_f32_16x16x32_bf16 v[68:71], a[4:7], v[60:63], 0\n\t"
    "v_mfma_f32_16x16x32_bf16 v[68:71], a[16:19], v[32:35], v[68:71]\n\t"
    "v_mfma_f32_16x16x32_bf16 v[68:71], a[28:31], v[36:39], v[68:71]\n\t"
    "v_mfma_f32_16x16x32_bf16 v[72:75], a[8:11], v[60:63], 0\n\t"
    "v_mfma_f32_16x16x32_bf16 v[72:75], a[20:23], v[32:35], v[72:75]\n\t"
    "v_mfma_f32_16x16x32_bf16 v[72:75], a[32:35], v[36:39], v[72:75]\n\t"
    "s_waitcnt lgkmcnt(2)\n\t"
    "v_mfma_f32_16x16x32_bf16 v[100:103], a[252:255], v[40:43], v[100:103]\n\t"
    "s_waitcnt lgkmcnt(1)\n\t"
    "v_mfma_f32_16x16x32_bf16 v[100:103], v[196:199], v[44:47], v[100:103]\n\t"
    "s_waitcnt lgkmcnt(0)\n\t"
    "v_mfma_f32_16x16x32_bf16 v[100:103], v[208:211], v[48:51], v[100:103]\n\t"
    "v_mfma_f32_16x16x32_bf16 v[104:107], v[188:191], v[40:43], v[104:107]\n\t"
    "v_mfma_f32_16x16x32_bf16 v[104:107], v[200:203], v[44:47], v[104:107]\n\t"
    "v_mfma_f32_16x16x32_bf16 v[104:107], v[212:215], v[48:51], v[104:107]\n\t"
    "v_mfma_f32_16x16x32_bf16 v[108:111], v[192:195], v[40:43], v[108:111]\n\t"
    "v_mfma_f32_16x16x32_bf16 v[108:111], v[204:207], v[44:47], v[108:111]\n\t"
    "v_mfma_f32_16x16x32_bf16 v[108:111], v[216:219], v[48:51], v[108:111]\n\t"
    "v_mfma_f32_16x16x32_bf16 v[136:139], a[144:147], v[40:43], v[136:139]\n\t"
    "v_mfma_f32_16x16x32_bf16 v[136:139], a[156:159], v[44:47], v[136:139]\n\t"
    "v_mfma_f32_16x16x32_bf16 v[136:139], a[168:171], v[48:51], v[136:139]\n\t"
    "v_mfma_f32_16x16x32_bf16 v[140:143], a[148:151], v[40:43], v[140:143]\n\t"
    "ds_read_b128 v[52:55], v15 offset:0\n\t"
    "v_mfma_f32_16x16x32_bf16 v[140:143], a[160:163], v[44:47], v[140:143]\n\t"
    "ds_read_b128 v[56:59], v15 offset:64\n\t"
    "v_mfma_f32_16x16x32_bf16 v[140:143], a[172:175], v[48:51], v[140:143]\n\t"
    "ds_read_b128 v[60:63], v15 offset:128\n\t"
    "v_mfma_f32_16x16x32_bf16 v[144:147], a[152:155], v[40:43], v[144:147]\n\t"
    "v_mfma_f32_16x16x32_bf16 v[144:147], a[164:167], v[44:47], v[144:147]\n\t"
    "v_mfma_f32_16x16x32_bf16 v[144:147], a[176:179], v[48:51], v[144:147]\n\t"
    "v_mfma_f32_16x16x32_bf16 v[64:67], a[36:39], v[40:43], v[64:67]\n\t"
    "v_mfma_f32_16x16x32_bf16 v[64:67], a[48:51], v[44:47], v[64:67]\n\t"
    "v_mfma_f32_16x16x32_bf16 v[64:67], a[60:63], v[48:51], v[64:67]\n\t"
    "v_mfma_f32_16x16x32_bf16 v[68:71], a[40:43], v[40:43], v[68:71]\n\t"
    "v_mfma_f32_16x16x32_bf16 v[68:71], a[52:55], v[44:47], v[68:71]\n\t"
    "v_mfma_f32_16x16x32_bf16 v[68:71], a[64:67], v[48:51], v[68:71]\n\t"
    "v_mfma_f32_16x16x32_bf16 v[72:75], a[44:47], v[40:43], v[72:75]\n\t"
    "v_mfma_f32_16x16x32_bf16 v[72:75], a[56:59], v[44:47], v[72:75]\n\t"
    "v_mfma_f32_16x16x32_bf16 v[72:75], a[68:71], v[48:51], v[72:75]\n\t"
    "s_waitcnt lgkmcnt(2)\n\t"
    "v_mfma_f32_16x16x32_bf16 v[100:103], v[220:223], v[52:55], v[100:103]\n\t"
    "s_waitcnt lgkmcnt(1)\n\t"
    "v_mfma_f32_16x16x32_bf16 v[100:103], v[232:235], v[56:59], v[100:103]\n\t"
    "s_waitcnt lgkmcnt(0)\n\t"
    "v_mfma_f32_16x16x32_bf16 v[100:103], v[244:247], v[60:63], v[100:103]\n\t"
    "v_mfma_f32_16x16x32_bf16 v[104:107], v[224:227], v[52:55], v[104:107]\n\t"
    "v_mfma_f32_16x16x32_bf16 v[104:107], v[236:239], v[56:59], v[104:107]\n\t"
    "v_mfma_f32_16x16x32_bf16 v[104:107], v[248:251], v[60:63], v[104:107]\n\t"
    "v_mfma_f32_16x16x32_bf16 v[108:111], v[228:231], v[52:55], v[108:111]\n\t"
    "v_mfma_f32_16x16x32_bf16 v[108:111], v[240:243], v[56:59], v[108:111]\n\t"
    "v_mfma_f32_16x16x32_bf16 v[108:111], v[252:255], v[60:63], v[108:111]\n\t"
    "v_mfma_f32_16x16x32_bf16 v[136:139], a[180:183], v[52:55], v[136:139]\n\t"
    "v_mfma_f32_16x16x32_bf16 v[136:139], a[192:195], v[56:59], v[136:139]\n\t"
    "v_mfma_f32_16x16x32_bf16 v[136:139], a[204:207], v[60:63], v[136:139]\n\t"
    "v_mfma_f32_16x16x32_bf16 v[140:143], a[184:187], v[52:55], v[140:143]\n\t"
    "ds_read_b128 v[32:35], v13 offset:3072\n\t"
    "v_mfma_f32_16x16x32_bf16 v[140:143], a[196:199], v[56:59], v[140:143]\n\t"
    "ds_read_b128 v[36:39], v13 offset:3136\n\t"
    "v_mfma_f32_16x16x32_bf16 v[140:143], a[208:211], v[60:63], v[140:143]\n\t"
    "ds_read_b128 v[40:43], v13 offset:3200\n\t"
    "v_mfma_f32_16x16x32_bf16 v[144:147], a[188:191], v[52:55], v[144:147]\n\t"
    "v_mfma_f32_16x16x32_bf16 v[144:147], a[200:203], v[56:59], v[144:147]\n\t"
    "s_add_u32 s78, s45, 0\n\t"
    "v_mfma_f32_16x16x32_bf16 v[144:147], a[212:215], v[60:63], v[144:147]\n\t"
    "v_cvt_pk_bf16_f32 v182, v108, v109\n\t"
    "v_mfma_f32_16x16x32_bf16 v[64:67], a[72:75], v[52:55], v[64:67]\n\t"
    "v_cvt_pk_bf16_f32 v183, v110, v111\n\t"
    "v_mfma_f32_16x16x32_bf16 v[64:67], a[84:87], v[56:59], v[64:67]\n\t"
    "ds_write_b64 v25, v[182:183] offset:3840\n\t"
    "v_mfma_f32_16x16x32_bf16 v[64:67], a[96:99], v[60:63], v[64:67]\n\t"
    "v_cvt_pk_bf16_f32 v178, v100, v101\n\t"
    "v_mfma_f32_16x16x32_bf16 v[68:71], a[76:79], v[52:55], v[68:71]\n\t"
    "v_cvt_pk_bf16_f32 v179, v102, v103\n\t"
    "v_mfma_f32_16x16x32_bf16 v[68:71], a[88:91], v[56:59], v[68:71]\n\t"
    "v_cvt_pk_bf16_f32 v180, v104, v105\n\t"
    "v_mfma_f32_16x16x32_bf16 v[68:71], a[100:103], v[60:63], v[68:71]\n\t"
    "v_cvt_pk_bf16_f32 v181, v106, v107\n\t"
    "v_mfma_f32_16x16x32_bf16 v[72:75], a[80:83], v[52:55], v[72:75]\n\t"
    "buffer_store_dwordx4 v[178:181], v22, s[28:31], s78 offen nt\n\t"
    "v_mfma_f32_16x16x32_bf16 v[72:75], a[92:95], v[56:59], v[72:75]\n\t"
    "v_mfma_f32_16x16x32_bf16 v[72:75], a[104:107], v[60:63], v[72:75]\n\t"
    "s_waitcnt lgkmcnt(3)\n\t"
    "v_mfma_f32_16x16x32_bf16 v[112:115], a[216:219], v[32:35], v[112:115]\n\t"
    "s_waitcnt lgkmcnt(2)\n\t"
    "v_mfma_f32_16x16x32_bf16 v[112:115], a[228:231], v[36:39], v[112:115]\n\t"
    "s_waitcnt lgkmcnt(0)\n\t"
    "s_barrier\n\t"
    "v_mfma_f32_16x16x32_bf16 v[112:115], a[240:243], v[40:43], v[112:115]\n\t"
    "ds_read_b128 v[28:31], v26 offset:2560\n\t"
    "v_mfma_f32_16x16x32_bf16 v[116:119], a[220:223], v[32:35], v[116:119]\n\t"
    "s_add_u32 s96, s44, 6144\n\t"
    "s_add_u32 s97, s45, 0\n\t"
    "v_mfma_f32_16x16x32_bf16 v[116:119], a[232:235], v[36:39], v[116:119]\n\t"
    "s_cmp_eq_u32 s98, 0\n\t"
    "s_cselect_b32 s96, s96, s97\n\t"
    "s_add_u32 m0, s53, 20480\n\t"
    "v_mfma_f32_16x16x32_bf16 v[116:119], a[244:247], v[40:43], v[116:119]\n\t"
    "buffer_load_dwordx4 v2, s[20:23], s41 offen lds\n\t"
    "v_mfma_f32_16x16x32_bf16 v[120:123], a[224:227], v[32:35], v[120:123]\n\t"
    "v_mfma_f32_16x16x32_bf16 v[120:123], a[236:239], v[36:39], v[120:123]\n\t"
    "v_mfma_f32_16x16x32_bf16 v[120:123], a[248:251], v[40:43], v[120:123]\n\t"
    "v_mfma_f32_16x16x32_bf16 v[148:151], a[108:111], v[32:35], v[148:151]\n\t"
    "v_mfma_f32_16x16x32_bf16 v[148:151], a[120:123], v[36:39], v[148:151]\n\t"
    "v_mfma_f32_16x16x32_bf16 v[148:151], a[132:135], v[40:43], v[148:151]\n\t"
    "v_mfma_f32_16x16x32_bf16 v[152:155], a[112:115], v[32:35], v[152:155]\n\t"
    "ds_read_b128 v[44:47], v14 offset:3072\n\t"
    "v_mfma_f32_16x16x32_bf16 v[152:155], a[124:127], v[36:39], v[152:155]\n\t"
    "ds_read_b128 v[48:51], v14 offset:3136\n\t"
    "v_mfma_f32_16x16x32_bf16 v[152:155], a[136:139], v[40:43], v[152:155]\n\t"
    "s_waitcnt lgkmcnt(2)\n\t"
    "buffer_store_dwordx4 v[28:31], v23, s[28:31], s96 offen nt\n\t"
    "ds_read_b128 v[52:55], v14 offset:3200\n\t"
    "v_mfma_f32_16x16x32_bf16 v[156:159], a[116:119], v[32:35], v[156:159]\n\t"
    "v_mfma_f32_16x16x32_bf16 v[156:159], a[128:131], v[36:39], v[156:159]\n\t"
    "v_mfma_f32_16x16x32_bf16 v[156:159], a[140:143], v[40:43], v[156:159]\n\t"
    "v_mfma_f32_16x16x32_bf16 v[76:79], a[0:3], v[32:35], 0\n\t"
    "v_mfma_f32_16x16x32_bf16 v[76:79], a[12:15], v[36:39], v[76:79]\n\t"
    "v_mfma_f32_16x16x32_bf16 v[76:79], a[24:27], v[40:43], v[76:79]\n\t"
    "v_mfma_f32_16x16x32_bf16 v[80:83], a[4:7], v[32:35], 0\n\t"
    "v_mfma_f32_16x16x32_bf16 v[80:83], a[16:19], v[36:39], v[80:83]\n\t"
    "v_mfma_f32_16x16x32_bf16 v[80:83], a[28:31], v[40:43], v[80:83]\n\t"
    "v_mfma_f32_16x16x32_bf16 v[84:87], a[8:11], v[32:35], 0\n\t"
    "v_mfma_f32_16x16x32_bf16 v[84:87], a[20:23], v[36:39], v[84:87]\n\t"
    "v_mfma_f32_16x16x32_bf16 v[84:87], a[32:35], v[40:43], v[84:87]\n\t"
    "s_waitcnt lgkmcnt(2)\n\t"
    "v_mfma_f32_16x16x32_bf16 v[112:115], a[252:255], v[44:47], v[112:115]\n\t"
    "s_waitcnt lgkmcnt(1)\n\t"
    "v_mfma_f32_16x16x32_bf16 v[112:115], v[196:199], v[48:51], v[112:115]\n\t"
    "s_waitcnt lgkmcnt(0)\n\t"
    "v_mfma_f32_16x16x32_bf16 v[112:115], v[208:211], v[52:55], v[112:115]\n\t"
    "v_mfma_f32_16x16x32_bf16 v[116:119], v[188:191], v[44:47], v[116:119]\n\t"
    "v_mfma_f32_16x16x32_bf16 v[116:119], v[200:203], v[48:51], v[116:119]\n\t"
    "v_mfma_f32_16x16x32_bf16 v[116:119], v[212:215], v[52:55], v[116:119]\n\t"
    "v_mfma_f32_16x16x32_bf16 v[120:123], v[192:195], v[44:47], v[120:123]\n\t"
    "v_mfma_f32_16x16x32_bf16 v[120:123], v[204:207], v[48:51], v[120:123]\n\t"
    "v_mfma_f32_16x16x32_bf16 v[120:123], v[216:219], v[52:55], v[120:123]\n\t"
    "v_mfma_f32_16x16x32_bf16 v[148:151], a[144:147], v[44:47], v[148:151]\n\t"
    "v_mfma_f32_16x16x32_bf16 v[148:151], a[156:159], v[48:51], v[148:151]\n\t"
    "v_mfma_f32_16x16x32_bf16 v[148:151], a[168:171], v[52:55], v[148:151]\n\t"
    "v_mfma_f32_16x16x32_bf16 v[152:155], a[148:151], v[44:47], v[152:155]\n\t"
    "ds_read_b128 v[56:59], v15 offset:3072\n\t"
    "v_mfma_f32_16x16x32_bf16 v[152:155], a[160:163], v[48:51], v[152:155]\n\t"
    "ds_read_b128 v[60:63], v15 offset:3136\n\t"
    "v_mfma_f32_16x16x32_bf16 v[152:155], a[172:175], v[52:55], v[152:155]\n\t"
    "ds_read_b128 v[32:35], v15 offset:3200\n\t"
    "v_mfma_f32_16x16x32_bf16 v[156:159], a[152:155], v[44:47], v[156:159]\n\t"
    "v_mfma_f32_16x16x32_bf16 v[156:159], a[164:167], v[48:51], v[156:159]\n\t"
    "v_mfma_f32_16x16x32_bf16 v[156:159], a[176:179], v[52:55], v[156:159]\n\t"
    "s_add_u32 m0, s53, 21504\n\t"
    "v_mfma_f32_16x16x32_bf16 v[76:79], a[36:39], v[44:47], v[76:79]\n\t"
    "buffer_load_dwordx4 v3, s[20:23], s41 offen lds\n\t"
    "v_mfma_f32_16x16x32_bf16 v[76:79], a[48:51], v[48:51], v[76:79]\n\t"
    "v_mfma_f32_16x16x32_bf16 v[76:79], a[60:63], v[52:55], v[76:79]\n\t"
    "v_mfma_f32_16x16x32_bf16 v[80:83], a[40:43], v[44:47], v[80:83]\n\t"
    "v_mfma_f32_16x16x32_bf16 v[80:83], a[52:55], v[48:51], v[80:83]\n\t"
    "v_mfma_f32_16x16x32_bf16 v[80:83], a[64:67], v[52:55], v[80:83]\n\t"
    "v_mfma_f32_16x16x32_bf16 v[84:87], a[44:47], v[44:47], v[84:87]\n\t"
    "v_mfma_f32_16x16x32_bf16 v[84:87], a[56:59], v[48:51], v[84:87]\n\t"
    "v_mfma_f32_16x16x32_bf16 v[84:87], a[68:71], v[52:55], v[84:87]\n\t"
    "s_waitcnt lgkmcnt(2)\n\t"
    "v_mfma_f32_16x16x32_bf16 v[112:115], v[220:223], v[56:59], v[112:115]\n\t"
    "s_waitcnt lgkmcnt(1)\n\t"
    "v_mfma_f32_16x16x32_bf16 v[112:115], v[232:235], v[60:63], v[112:115]\n\t"
    "s_waitcnt lgkmcnt(0)\n\t"
    "v_mfma_f32_16x16x32_bf16 v[112:115], v[244:247], v[32:35], v[112:115]\n\t"
    "v_mfma_f32_16x16x32_bf16 v[116:119], v[224:227], v[56:59], v[116:119]\n\t"
    "v_mfma_f32_16x16x32_bf16 v[116:119], v[236:239], v[60:63], v[116:119]\n\t"
    "v_mfma_f32_16x16x32_bf16 v[116:119], v[248:251], v[32:35], v[116:119]\n\t"
    "v_mfma_f32_16x16x32_bf16 v[120:123], v[228:231], v[56:59], v[120:123]\n\t"
    "v_mfma_f32_16x16x32_bf16 v[120:123], v[240:243], v[60:63], v[120:123]\n\t"
    "v_mfma_f32_16x16x32_bf16 v[120:123], v[252:255], v[32:35], v[120:123]\n\t"
    "v_mfma_f32_16x16x32_bf16 v[148:151], a[180:183], v[56:59], v[148:151]\n\t"
    "v_mfma_f32_16x16x32_bf16 v[148:151], a[192:195], v[60:63], v[148:151]\n\t"
    "v_mfma_f32_16x16x32_bf16 v[148:151], a[204:207], v[32:35], v[148:151]\n\t"
    "v_mfma_f32_16x16x32_bf16 v[152:155], a[184:187], v[56:59], v[152:155]\n\t"
    "ds_read_b128 v[36:39], v13 offset:6144\n\t"
    "v_mfma_f32_16x16x32_bf16 v[152:155], a[196:199], v[60:63], v[152:155]\n\t"
    "ds_read_b128 v[40:43], v13 offset:6208\n\t"
    "v_mfma_f32_16x16x32_bf16 v[152:155], a[208:211], v[32:35], v[152:155]\n\t"
    "ds_read_b128 v[44:47], v13 offset:6272\n\t"
    "v_mfma_f32_16x16x32_bf16 v[156:159], a[188:191], v[56:59], v[156:159]\n\t"
    "v_mfma_f32_16x16x32_bf16 v[156:159], a[200:203], v[60:63], v[156:159]\n\t"
    "s_add_u32 s77, s45, 3072\n\t"
    "v_mfma_f32_16x16x32_bf16 v[156:159], a[212:215], v[32:35], v[156:159]\n\t"
    "v_cvt_pk_bf16_f32 v176, v120, v121\n\t"
    "v_mfma_f32_16x16x32_bf16 v[76:79], a[72:75], v[56:59], v[76:79]\n\t"
    "v_cvt_pk_bf16_f32 v177, v122, v123\n\t"
    "v_mfma_f32_16x16x32_bf16 v[76:79], a[84:87], v[60:63], v[76:79]\n\t"
    "ds_write_b64 v25, v[176:177] offset:0\n\t"
    "v_mfma_f32_16x16x32_bf16 v[76:79], a[96:99], v[32:35], v[76:79]\n\t"
    "v_cvt_pk_bf16_f32 v172, v112, v113\n\t"
    "v_mfma_f32_16x16x32_bf16 v[80:83], a[76:79], v[56:59], v[80:83]\n\t"
    "v_cvt_pk_bf16_f32 v173, v114, v115\n\t"
    "v_mfma_f32_16x16x32_bf16 v[80:83], a[88:91], v[60:63], v[80:83]\n\t"
    "v_cvt_pk_bf16_f32 v174, v116, v117\n\t"
    "v_mfma_f32_16x16x32_bf16 v[80:83], a[100:103], v[32:35], v[80:83]\n\t"
    "v_cvt_pk_bf16_f32 v175, v118, v119\n\t"
    "v_mfma_f32_16x16x32_bf16 v[84:87], a[80:83], v[56:59], v[84:87]\n\t"
    "buffer_store_dwordx4 v[172:175], v22, s[28:31], s77 offen nt\n\t"
    "v_mfma_f32_16x16x32_bf16 v[84:87], a[92:95], v[60:63], v[84:87]\n\t"
    "v_mfma_f32_16x16x32_bf16 v[84:87], a[104:107], v[32:35], v[84:87]\n\t"
    "s_waitcnt lgkmcnt(3)\n\t"
    "v_mfma_f32_16x16x32_bf16 v[124:127], a[216:219], v[36:39], v[124:127]\n\t"
    "s_waitcnt lgkmcnt(2)\n\t"
    "v_mfma_f32_16x16x32_bf16 v[124:127], a[228:231], v[40:43], v[124:127]\n\t"
    "s_waitcnt lgkmcnt(1)\n\t"
    "v_mfma_f32_16x16x32_bf16 v[124:127], a[240:243], v[44:47], v[124:127]\n\t"
    "v_mfma_f32_16x16x32_bf16 v[128:131], a[220:223], v[36:39], v[128:131]\n\t"
    "s_add_u32 m0, s53, 22528\n\t"
    "v_mfma_f32_16x16x32_bf16 v[128:131], a[232:235], v[40:43], v[128:131]\n\t"
    "buffer_load_dwordx4 v4, s[20:23], s41 offen lds\n\t"
    "v_mfma_f32_16x16x32_bf16 v[128:131], a[244:247], v[44:47], v[128:131]\n\t"
    "v_mfma_f32_16x16x32_bf16 v[132:135], a[224:227], v[36:39], v[132:135]\n\t"
    "v_mfma_f32_16x16x32_bf16 v[132:135], a[236:239], v[40:43], v[132:135]\n\t"
    "v_mfma_f32_16x16x32_bf16 v[132:135], a[248:251], v[44:47], v[132:135]\n\t"
    "v_mfma_f32_16x16x32_bf16 v[160:163], a[108:111], v[36:39], v[160:163]\n\t"
    "v_mfma_f32_16x16x32_bf16 v[160:163], a[120:123], v[40:43], v[160:163]\n\t"
    "v_mfma_f32_16x16x32_bf16 v[160:163], a[132:135], v[44:47], v[160:163]\n\t"
    "v_mfma_f32_16x16x32_bf16 v[164:167], a[112:115], v[36:39], v[164:167]\n\t"
    "ds_read_b128 v[48:51], v14 offset:6144\n\t"
    "v_mfma_f32_16x16x32_bf16 v[164:167], a[124:127], v[40:43], v[164:167]\n\t"
    "ds_read_b128 v[52:55], v14 offset:6208\n\t"
    "v_mfma_f32_16x16x32_bf16 v[164:167], a[136:139], v[44:47], v[164:167]\n\t"
    "ds_read_b128 v[56:59], v14 offset:6272\n\t"
    "v_mfma_f32_16x16x32_bf16 v[168:171], a[116:119], v[36:39], v[168:171]\n\t"
    "v_mfma_f32_16x16x32_bf16 v[168:171], a[128:131], v[40:43], v[168:171]\n\t"
    "v_mfma_f32_16x16x32_bf16 v[168:171], a[140:143], v[44:47], v[168:171]\n\t"
    "v_mfma_f32_16x16x32_bf16 v[88:91], a[0:3], v[36:39], 0\n\t"
    "v_mfma_f32_16x16x32_bf16 v[88:91], a[12:15], v[40:43], v[88:91]\n\t"
    "v_mfma_f32_16x16x32_bf16 v[88:91], a[24:27], v[44:47], v[88:91]\n\t"
    "v_mfma_f32_16x16x32_bf16 v[92:95], a[4:7], v[36:39], 0\n\t"
    "v_mfma_f32_16x16x32_bf16 v[92:95], a[16:19], v[40:43], v[92:95]\n\t"
    "v_mfma_f32_16x16x32_bf16 v[92:95], a[28:31], v[44:47], v[92:95]\n\t"
    "v_mfma_f32_16x16x32_bf16 v[96:99], a[8:11], v[36:39], 0\n\t"
    "v_mfma_f32_16x16x32_bf16 v[96:99], a[20:23], v[40:43], v[96:99]\n\t"
    "v_mfma_f32_16x16x32_bf16 v[96:99], a[32:35], v[44:47], v[96:99]\n\t"
    "s_waitcnt lgkmcnt(2)\n\t"
    "v_mfma_f32_16x16x32_bf16 v[124:127], a[252:255], v[48:51], v[124:127]\n\t"
    "s_waitcnt lgkmcnt(1)\n\t"
    "v_mfma_f32_16x16x32_bf16 v[124:127], v[196:199], v[52:55], v[124:127]\n\t"
    "s_waitcnt lgkmcnt(0)\n\t"
    "v_mfma_f32_16x16x32_bf16 v[124:127], v[208:211], v[56:59], v[124:127]\n\t"
    "v_mfma_f32_16x16x32_bf16 v[128:131], v[188:191], v[48:51], v[128:131]\n\t"
    "v_mfma_f32_16x16x32_bf16 v[128:131], v[200:203], v[52:55], v[128:131]\n\t"
    "v_mfma_f32_16x16x32_bf16 v[128:131], v[212:215], v[56:59], v[128:131]\n\t"
    "v_mfma_f32_16x16x32_bf16 v[132:135], v[192:195], v[48:51], v[132:135]\n\t"
    "v_mfma_f32_16x16x32_bf16 v[132:135], v[204:207], v[52:55], v[132:135]\n\t"
    "v_mfma_f32_16x16x32_bf16 v[132:135], v[216:219], v[56:59], v[132:135]\n\t"
    "v_mfma_f32_16x16x32_bf16 v[160:163], a[144:147], v[48:51], v[160:163]\n\t"
    "v_mfma_f32_16x16x32_bf16 v[160:163], a[156:159], v[52:55], v[160:163]\n\t"
    "v_mfma_f32_16x16x32_bf16 v[160:163], a[168:171], v[56:59], v[160:163]\n\t"
    "v_mfma_f32_16x16x32_bf16 v[164:167], a[148:151], v[48:51], v[164:167]\n\t"
    "ds_read_b128 v[60:63], v15 offset:6144\n\t"
    "v_mfma_f32_16x16x32_bf16 v[164:167], a[160:163], v[52:55], v[164:167]\n\t"
    "ds_read_b128 v[32:35], v15 offset:6208\n\t"
    "v_mfma_f32_16x16x32_bf16 v[164:167], a[172:175], v[56:59], v[164:167]\n\t"
    "ds_read_b128 v[36:39], v15 offset:6272\n\t"
    "v_mfma_f32_16x16x32_bf16 v[168:171], a[152:155], v[48:51], v[168:171]\n\t"
    "v_mfma_f32_16x16x32_bf16 v[168:171], a[164:167], v[52:55], v[168:171]\n\t"
    "s_add_u32 m0, s53, 23552\n\t"
    "v_mfma_f32_16x16x32_bf16 v[168:171], a[176:179], v[56:59], v[168:171]\n\t"
    "buffer_load_dwordx4 v5, s[20:23], s41 offen lds\n\t"
    "v_mfma_f32_16x16x32_bf16 v[88:91], a[36:39], v[48:51], v[88:91]\n\t"
    "v_mfma_f32_16x16x32_bf16 v[88:91], a[48:51], v[52:55], v[88:91]\n\t"
    "v_mfma_f32_16x16x32_bf16 v[88:91], a[60:63], v[56:59], v[88:91]\n\t"
    "v_mfma_f32_16x16x32_bf16 v[92:95], a[40:43], v[48:51], v[92:95]\n\t"
    "v_mfma_f32_16x16x32_bf16 v[92:95], a[52:55], v[52:55], v[92:95]\n\t"
    "v_mfma_f32_16x16x32_bf16 v[92:95], a[64:67], v[56:59], v[92:95]\n\t"
    "v_mfma_f32_16x16x32_bf16 v[96:99], a[44:47], v[48:51], v[96:99]\n\t"
    "v_mfma_f32_16x16x32_bf16 v[96:99], a[56:59], v[52:55], v[96:99]\n\t"
    "v_mfma_f32_16x16x32_bf16 v[96:99], a[68:71], v[56:59], v[96:99]\n\t"
    "s_waitcnt lgkmcnt(2)\n\t"
    "v_mfma_f32_16x16x32_bf16 v[124:127], v[220:223], v[60:63], v[124:127]\n\t"
    "s_waitcnt lgkmcnt(1)\n\t"
    "v_mfma_f32_16x16x32_bf16 v[124:127], v[232:235], v[32:35], v[124:127]\n\t"
    "s_waitcnt lgkmcnt(0)\n\t"
    "v_mfma_f32_16x16x32_bf16 v[124:127], v[244:247], v[36:39], v[124:127]\n\t"
    "v_mfma_f32_16x16x32_bf16 v[128:131], v[224:227], v[60:63], v[128:131]\n\t"
    "v_mfma_f32_16x16x32_bf16 v[128:131], v[236:239], v[32:35], v[128:131]\n\t"
    "v_mfma_f32_16x16x32_bf16 v[128:131], v[248:251], v[36:39], v[128:131]\n\t"
    "v_mfma_f32_16x16x32_bf16 v[132:135], v[228:231], v[60:63], v[132:135]\n\t"
    "v_mfma_f32_16x16x32_bf16 v[132:135], v[240:243], v[32:35], v[132:135]\n\t"
    "v_mfma_f32_16x16x32_bf16 v[132:135], v[252:255], v[36:39], v[132:135]\n\t"
    "v_mfma_f32_16x16x32_bf16 v[160:163], a[180:183], v[60:63], v[160:163]\n\t"
    "v_mfma_f32_16x16x32_bf16 v[160:163], a[192:195], v[32:35], v[160:163]\n\t"
    "s_waitcnt vmcnt(9)\n\t"
    "s_barrier\n\t"
    "v_mfma_f32_16x16x32_bf16 v[160:163], a[204:207], v[36:39], v[160:163]\n\t"
    "v_mfma_f32_16x16x32_bf16 v[164:167], a[184:187], v[60:63], v[164:167]\n\t"
    "ds_read_b128 v[40:43], v13 offset:20480\n\t"
    "v_mfma_f32_16x16x32_bf16 v[164:167], a[196:199], v[32:35], v[164:167]\n\t"
    "ds_read_b128 v[44:47], v13 offset:20544\n\t"
    "v_mfma_f32_16x16x32_bf16 v[164:167], a[208:211], v[36:39], v[164:167]\n\t"
    "ds_read_b128 v[48:51], v13 offset:20608\n\t"
    "v_mfma_f32_16x16x32_bf16 v[168:171], a[188:191], v[60:63], v[168:171]\n\t"
    "v_mfma_f32_16x16x32_bf16 v[168:171], a[200:203], v[32:35], v[168:171]\n\t"
    "s_add_u32 s78, s45, 6144\n\t"
    "v_mfma_f32_16x16x32_bf16 v[168:171], a[212:215], v[36:39], v[168:171]\n\t"
    "v_cvt_pk_bf16_f32 v182, v132, v133\n\t"
    "v_mfma_f32_16x16x32_bf16 v[88:91], a[72:75], v[60:63], v[88:91]\n\t"
    "v_cvt_pk_bf16_f32 v183, v134, v135\n\t"
    "v_mfma_f32_16x16x32_bf16 v[88:91], a[84:87], v[32:35], v[88:91]\n\t"
    "ds_write_b64 v25, v[182:183] offset:1280\n\t"
    "v_mfma_f32_16x16x32_bf16 v[88:91], a[96:99], v[36:39], v[88:91]\n\t"
    "v_cvt_pk_bf16_f32 v178, v124, v125\n\t"
    "v_mfma_f32_16x16x32_bf16 v[92:95], a[76:79], v[60:63], v[92:95]\n\t"
    "v_cvt_pk_bf16_f32 v179, v126, v127\n\t"
    "v_mfma_f32_16x16x32_bf16 v[92:95], a[88:91], v[32:35], v[92:95]\n\t"
    "v_cvt_pk_bf16_f32 v180, v128, v129\n\t"
    "v_mfma_f32_16x16x32_bf16 v[92:95], a[100:103], v[36:39], v[92:95]\n\t"
    "v_cvt_pk_bf16_f32 v181, v130, v131\n\t"
    "v_mfma_f32_16x16x32_bf16 v[96:99], a[80:83], v[60:63], v[96:99]\n\t"
    "buffer_store_dwordx4 v[178:181], v22, s[28:31], s78 offen nt\n\t"
    "v_mfma_f32_16x16x32_bf16 v[96:99], a[92:95], v[32:35], v[96:99]\n\t"
    "v_mfma_f32_16x16x32_bf16 v[96:99], a[104:107], v[36:39], v[96:99]\n\t"
    "s_waitcnt lgkmcnt(3)\n\t"
    "v_mfma_f32_16x16x32_bf16 v[136:139], a[216:219], v[40:43], v[136:139]\n\t"
    "s_waitcnt lgkmcnt(2)\n\t"
    "v_mfma_f32_16x16x32_bf16 v[136:139], a[228:231], v[44:47], v[136:139]\n\t"
    "s_waitcnt lgkmcnt(0)\n\t"
    "s_barrier\n\t"
    "v_mfma_f32_16x16x32_bf16 v[136:139], a[240:243], v[48:51], v[136:139]\n\t"
    "ds_read_b128 v[28:31], v26 offset:0\n\t"
    "s_add_u32 m0, s53, 24576\n\t"
    "v_mfma_f32_16x16x32_bf16 v[140:143], a[220:223], v[40:43], v[140:143]\n\t"
    "s_add_u32 s96, s45, 3072\n\t"
    "s_add_u32 s97, s45, 6144\n\t"
    "buffer_load_dwordx4 v6, s[20:23], s41 offen lds\n\t"
    "v_mfma_f32_16x16x32_bf16 v[140:143], a[232:235], v[44:47], v[140:143]\n\t"
    "s_cmp_eq_u32 s98, 0\n\t"
    "s_cselect_b32 s96, s96, s97\n\t"
    "v_mfma_f32_16x16x32_bf16 v[140:143], a[244:247], v[48:51], v[140:143]\n\t"
    "v_mfma_f32_16x16x32_bf16 v[144:147], a[224:227], v[40:43], v[144:147]\n\t"
    "v_mfma_f32_16x16x32_bf16 v[144:147], a[236:239], v[44:47], v[144:147]\n\t"
    "v_mfma_f32_16x16x32_bf16 v[144:147], a[248:251], v[48:51], v[144:147]\n\t"
    "v_mfma_f32_16x16x32_bf16 v[64:67], a[108:111], v[40:43], v[64:67]\n\t"
    "v_mfma_f32_16x16x32_bf16 v[64:67], a[120:123], v[44:47], v[64:67]\n\t"
    "v_mfma_f32_16x16x32_bf16 v[64:67], a[132:135], v[48:51], v[64:67]\n\t"
    "v_mfma_f32_16x16x32_bf16 v[68:71], a[112:115], v[40:43], v[68:71]\n\t"
    "ds_read_b128 v[52:55], v14 offset:20480\n\t"
    "v_mfma_f32_16x16x32_bf16 v[68:71], a[124:127], v[44:47], v[68:71]\n\t"
    "ds_read_b128 v[56:59], v14 offset:20544\n\t"
    "v_mfma_f32_16x16x32_bf16 v[68:71], a[136:139], v[48:51], v[68:71]\n\t"
    "s_waitcnt lgkmcnt(2)\n\t"
    "buffer_store_dwordx4 v[28:31], v23, s[28:31], s96 offen nt\n\t"
    "ds_read_b128 v[60:63], v14 offset:20608\n\t"
    "v_mfma_f32_16x16x32_bf16 v[72:75], a[116:119], v[40:43], v[72:75]\n\t"
    "v_mfma_f32_16x16x32_bf16 v[72:75], a[128:131], v[44:47], v[72:75]\n\t"
    "v_mfma_f32_16x16x32_bf16 v[72:75], a[140:143], v[48:51], v[72:75]\n\t"
    "v_mfma_f32_16x16x32_bf16 v[100:103], a[0:3], v[40:43], 0\n\t"
    "v_mfma_f32_16x16x32_bf16 v[100:103], a[12:15], v[44:47], v[100:103]\n\t"
    "v_mfma_f32_16x16x32_bf16 v[100:103], a[24:27], v[48:51], v[100:103]\n\t"
    "v_mfma_f32_16x16x32_bf16 v[104:107], a[4:7], v[40:43], 0\n\t"
    "v_mfma_f32_16x16x32_bf16 v[104:107], a[16:19], v[44:47], v[104:107]\n\t"
    "v_mfma_f32_16x16x32_bf16 v[104:107], a[28:31], v[48:51], v[104:107]\n\t"
    "v_mfma_f32_16x16x32_bf16 v[108:111], a[8:11], v[40:43], 0\n\t"
    "v_mfma_f32_16x16x32_bf16 v[108:111], a[20:23], v[44:47], v[108:111]\n\t"
    "v_mfma_f32_16x16x32_bf16 v[108:111], a[32:35], v[48:51], v[108:111]\n\t"
    "s_waitcnt lgkmcnt(2)\n\t"
    "v_mfma_f32_16x16x32_bf16 v[136:139], a[252:255], v[52:55], v[136:139]\n\t"
    "s_waitcnt lgkmcnt(1)\n\t"
    "v_mfma_f32_16x16x32_bf16 v[136:139], v[196:199], v[56:59], v[136:139]\n\t"
    "s_waitcnt lgkmcnt(0)\n\t"
    "v_mfma_f32_16x16x32_bf16 v[136:139], v[208:211], v[60:63], v[136:139]\n\t"
    "v_mfma_f32_16x16x32_bf16 v[140:143], v[188:191], v[52:55], v[140:143]\n\t"
    "v_mfma_f32_16x16x32_bf16 v[140:143], v[200:203], v[56:59], v[140:143]\n\t"
    "v_mfma_f32_16x16x32_bf16 v[140:143], v[212:215], v[60:63], v[140:143]\n\t"
    "v_mfma_f32_16x16x32_bf16 v[144:147], v[192:195], v[52:55], v[144:147]\n\t"
    "v_mfma_f32_16x16x32_bf16 v[144:147], v[204:207], v[56:59], v[144:147]\n\t"
    "v_mfma_f32_16x16x32_bf16 v[144:147], v[216:219], v[60:63], v[144:147]\n\t"
    "v_mfma_f32_16x16x32_bf16 v[64:67], a[144:147], v[52:55], v[64:67]\n\t"
    "v_mfma_f32_16x16x32_bf16 v[64:67], a[156:159], v[56:59], v[64:67]\n\t"
    "v_mfma_f32_16x16x32_bf16 v[64:67], a[168:171], v[60:63], v[64:67]\n\t"
    "v_mfma_f32_16x16x32_bf16 v[68:71], a[148:151], v[52:55], v[68:71]\n\t"
    "ds_read_b128 v[32:35], v15 offset:20480\n\t"
    "v_mfma_f32_16x16x32_bf16 v[68:71], a[160:163], v[56:59], v[68:71]\n\t"
    "ds_read_b128 v[36:39], v15 offset:20544\n\t"
    "v_mfma_f32_16x16x32_bf16 v[68:71], a[172:175], v[60:63], v[68:71]\n\t"
    "ds_read_b128 v[40:43], v15 offset:20608\n\t"
    "v_mfma_f32_16x16x32_bf16 v[72:75], a[152:155], v[52:55], v[72:75]\n\t"
    "v_mfma_f32_16x16x32_bf16 v[72:75], a[164:167], v[56:59], v[72:75]\n\t"
    "v_mfma_f32_16x16x32_bf16 v[72:75], a[176:179], v[60:63], v[72:75]\n\t"
    "v_mfma_f32_16x16x32_bf16 v[100:103], a[36:39], v[52:55], v[100:103]\n\t"
    "v_mfma_f32_16x16x32_bf16 v[100:103], a[48:51], v[56:59], v[100:103]\n\t"
    "v_mfma_f32_16x16x32_bf16 v[100:103], a[60:63], v[60:63], v[100:103]\n\t"
    "v_mfma_f32_16x16x32_bf16 v[104:107], a[40:43], v[52:55], v[104:107]\n\t"
    "v_mfma_f32_16x16x32_bf16 v[104:107], a[52:55], v[56:59], v[104:107]\n\t"
    "v_mfma_f32_16x16x32_bf16 v[104:107], a[64:67], v[60:63], v[104:107]\n\t"
    "v_mfma_f32_16x16x32_bf16 v[108:111], a[44:47], v[52:55], v[108:111]\n\t"
    "v_mfma_f32_16x16x32_bf16 v[108:111], a[56:59], v[56:59], v[108:111]\n\t"
    "v_mfma_f32_16x16x32_bf16 v[108:111], a[68:71], v[60:63], v[108:111]\n\t"
    "s_waitcnt lgkmcnt(2)\n\t"
    "v_mfma_f32_16x16x32_bf16 v[136:139], v[220:223], v[32:35], v[136:139]\n\t"
    "s_waitcnt lgkmcnt(1)\n\t"
    "v_mfma_f32_16x16x32_bf16 v[136:139], v[232:235], v[36:39], v[136:139]\n\t"
    "s_waitcnt lgkmcnt(0)\n\t"
    "v_mfma_f32_16x16x32_bf16 v[136:139], v[244:247], v[40:43], v[136:139]\n\t"
    "v_mfma_f32_16x16x32_bf16 v[140:143], v[224:227], v[32:35], v[140:143]\n\t"
    "v_mfma_f32_16x16x32_bf16 v[140:143], v[236:239], v[36:39], v[140:143]\n\t"
    "v_mfma_f32_16x16x32_bf16 v[140:143], v[248:251], v[40:43], v[140:143]\n\t"
    "v_mfma_f32_16x16x32_bf16 v[144:147], v[228:231], v[32:35], v[144:147]\n\t"
    "v_mfma_f32_16x16x32_bf16 v[144:147], v[240:243], v[36:39], v[144:147]\n\t"
    "v_mfma_f32_16x16x32_bf16 v[144:147], v[252:255], v[40:43], v[144:147]\n\t"
    "v_mfma_f32_16x16x32_bf16 v[64:67], a[180:183], v[32:35], v[64:67]\n\t"
    "v_mfma_f32_16x16x32_bf16 v[64:67], a[192:195], v[36:39], v[64:67]\n\t"
    "v_mfma_f32_16x16x32_bf16 v[64:67], a[204:207], v[40:43], v[64:67]\n\t"
    "v_mfma_f32_16x16x32_bf16 v[68:71], a[184:187], v[32:35], v[68:71]\n\t"
    "ds_read_b128 v[44:47], v13 offset:23552\n\t"
    "v_mfma_f32_16x16x32_bf16 v[68:71], a[196:199], v[36:39], v[68:71]\n\t"
    "ds_read_b128 v[48:51], v13 offset:23616\n\t"
    "v_mfma_f32_16x16x32_bf16 v[68:71], a[208:211], v[40:43], v[68:71]\n\t"
    "ds_read_b128 v[52:55], v13 offset:23680\n\t"
    "v_mfma_f32_16x16x32_bf16 v[72:75], a[188:191], v[32:35], v[72:75]\n\t"
    "v_mfma_f32_16x16x32_bf16 v[72:75], a[200:203], v[36:39], v[72:75]\n\t"
    "s_add_u32 s77, s46, 0\n\t"
    "v_mfma_f32_16x16x32_bf16 v[72:75], a[212:215], v[40:43], v[72:75]\n\t"
    "v_cvt_pk_bf16_f32 v176, v144, v145\n\t"
    "v_mfma_f32_16x16x32_bf16 v[100:103], a[72:75], v[32:35], v[100:103]\n\t"
    "v_cvt_pk_bf16_f32 v177, v146, v147\n\t"
    "v_mfma_f32_16x16x32_bf16 v[100:103], a[84:87], v[36:39], v[100:103]\n\t"
    "ds_write_b64 v25, v[176:177] offset:2560\n\t"
    "v_mfma_f32_16x16x32_bf16 v[100:103], a[96:99], v[40:43], v[100:103]\n\t"
    "v_cvt_pk_bf16_f32 v172, v136, v137\n\t"
    "v_mfma_f32_16x16x32_bf16 v[104:107], a[76:79], v[32:35], v[104:107]\n\t"
    "v_cvt_pk_bf16_f32 v173, v138, v139\n\t"
    "v_mfma_f32_16x16x32_bf16 v[104:107], a[88:91], v[36:39], v[104:107]\n\t"
    "v_cvt_pk_bf16_f32 v174, v140, v141\n\t"
    "v_mfma_f32_16x16x32_bf16 v[104:107], a[100:103], v[40:43], v[104:107]\n\t"
    "v_cvt_pk_bf16_f32 v175, v142, v143\n\t"
    "v_mfma_f32_16x16x32_bf16 v[108:111], a[80:83], v[32:35], v[108:111]\n\t"
    "buffer_store_dwordx4 v[172:175], v22, s[28:31], s77 offen nt\n\t"
    "v_mfma_f32_16x16x32_bf16 v[108:111], a[92:95], v[36:39], v[108:111]\n\t"
    "v_mfma_f32_16x16x32_bf16 v[108:111], a[104:107], v[40:43], v[108:111]\n\t"
    "s_waitcnt lgkmcnt(3)\n\t"
    "v_mfma_f32_16x16x32_bf16 v[148:151], a[216:219], v[44:47], v[148:151]\n\t"
    "s_waitcnt lgkmcnt(2)\n\t"
    "v_mfma_f32_16x16x32_bf16 v[148:151], a[228:231], v[48:51], v[148:151]\n\t"
    "s_waitcnt lgkmcnt(1)\n\t"
    "v_mfma_f32_16x16x32_bf16 v[148:151], a[240:243], v[52:55], v[148:151]\n\t"
    "v_mfma_f32_16x16x32_bf16 v[152:155], a[220:223], v[44:47], v[152:155]\n\t"
    "v_mfma_f32_16x16x32_bf16 v[152:155], a[232:235], v[48:51], v[152:155]\n\t"
    "s_add_u32 m0, s53, 40960\n\t"
    "v_mfma_f32_16x16x32_bf16 v[152:155], a[244:247], v[52:55], v[152:155]\n\t"
    "buffer_load_dwordx4 v2, s[20:23], s42 offen lds\n\t"
    "v_mfma_f32_16x16x32_bf16 v[156:159], a[224:227], v[44:47], v[156:159]\n\t"
    "v_mfma_f32_16x16x32_bf16 v[156:159], a[236:239], v[48:51], v[156:159]\n\t"
    "v_mfma_f32_16x16x32_bf16 v[156:159], a[248:251], v[52:55], v[156:159]\n\t"
    "v_mfma_f32_16x16x32_bf16 v[76:79], a[108:111], v[44:47], v[76:79]\n\t"
    "v_mfma_f32_16x16x32_bf16 v[76:79], a[120:123], v[48:51], v[76:79]\n\t"
    "v_mfma_f32_16x16x32_bf16 v[76:79], a[132:135], v[52:55], v[76:79]\n\t"
    "v_mfma_f32_16x16x32_bf16 v[80:83], a[112:115], v[44:47], v[80:83]\n\t"
    "ds_read_b128 v[56:59], v14 offset:23552\n\t"
    "v_mfma_f32_16x16x32_bf16 v[80:83], a[124:127], v[48:51], v[80:83]\n\t"
    "ds_read_b128 v[60:63], v14 offset:23616\n\t"
    "v_mfma_f32_16x16x32_bf16 v[80:83], a[136:139], v[52:55], v[80:83]\n\t"
    "ds_read_b128 v[32:35], v14 offset:23680\n\t"
    "v_mfma_f32_16x16x32_bf16 v[84:87], a[116:119], v[44:47], v[84:87]\n\t"
    "v_mfma_f32_16x16x32_bf16 v[84:87], a[128:131], v[48:51], v[84:87]\n\t"
    "v_mfma_f32_16x16x32_bf16 v[84:87], a[140:143], v[52:55], v[84:87]\n\t"
    "v_mfma_f32_16x16x32_bf16 v[112:115], a[0:3], v[44:47], 0\n\t"
    "v_mfma_f32_16x16x32_bf16 v[112:115], a[12:15], v[48:51], v[112:115]\n\t"
    "v_mfma_f32_16x16x32_bf16 v[112:115], a[24:27], v[52:55], v[112:115]\n\t"
    "v_mfma_f32_16x16x32_bf16 v[116:119], a[4:7], v[44:47], 0\n\t"
    "v_mfma_f32_16x16x32_bf16 v[116:119], a[16:19], v[48:51], v[116:119]\n\t"
    "v_mfma_f32_16x16x32_bf16 v[116:119], a[28:31], v[52:55], v[116:119]\n\t"
    "v_mfma_f32_16x16x32_bf16 v[120:123], a[8:11], v[44:47], 0\n\t"
    "v_mfma_f32_16x16x32_bf16 v[120:123], a[20:23], v[48:51], v[120:123]\n\t"
    "v_mfma_f32_16x16x32_bf16 v[120:123], a[32:35], v[52:55], v[120:123]\n\t"
    "s_waitcnt lgkmcnt(2)\n\t"
    "v_mfma_f32_16x16x32_bf16 v[148:151], a[252:255], v[56:59], v[148:151]\n\t"
    "s_waitcnt lgkmcnt(1)\n\t"
    "v_mfma_f32_16x16x32_bf16 v[148:151], v[196:199], v[60:63], v[148:151]\n\t"
    "s_waitcnt lgkmcnt(0)\n\t"
    "v_mfma_f32_16x16x32_bf16 v[148:151], v[208:211], v[32:35], v[148:151]\n\t"
    "v_mfma_f32_16x16x32_bf16 v[152:155], v[188:191], v[56:59], v[152:155]\n\t"
    "v_mfma_f32_16x16x32_bf16 v[152:155], v[200:203], v[60:63], v[152:155]\n\t"
    "v_mfma_f32_16x16x32_bf16 v[152:155], v[212:215], v[32:35], v[152:155]\n\t"
    "v_mfma_f32_16x16x32_bf16 v[156:159], v[192:195], v[56:59], v[156:159]\n\t"
    "v_mfma_f32_16x16x32_bf16 v[156:159], v[204:207], v[60:63], v[156:159]\n\t"
    "v_mfma_f32_16x16x32_bf16 v[156:159], v[216:219], v[32:35], v[156:159]\n\t"
    "v_mfma_f32_16x16x32_bf16 v[76:79], a[144:147], v[56:59], v[76:79]\n\t"
    "v_mfma_f32_16x16x32_bf16 v[76:79], a[156:159], v[60:63], v[76:79]\n\t"
    "v_mfma_f32_16x16x32_bf16 v[76:79], a[168:171], v[32:35], v[76:79]\n\t"
    "v_mfma_f32_16x16x32_bf16 v[80:83], a[148:151], v[56:59], v[80:83]\n\t"
    "ds_read_b128 v[36:39], v15 offset:23552\n\t"
    "v_mfma_f32_16x16x32_bf16 v[80:83], a[160:163], v[60:63], v[80:83]\n\t"
    "ds_read_b128 v[40:43], v15 offset:23616\n\t"
    "v_mfma_f32_16x16x32_bf16 v[80:83], a[172:175], v[32:35], v[80:83]\n\t"
    "ds_read_b128 v[44:47], v15 offset:23680\n\t"
    "v_mfma_f32_16x16x32_bf16 v[84:87], a[152:155], v[56:59], v[84:87]\n\t"
    "v_mfma_f32_16x16x32_bf16 v[84:87], a[164:167], v[60:63], v[84:87]\n\t"
    "v_mfma_f32_16x16x32_bf16 v[84:87], a[176:179], v[32:35], v[84:87]\n\t"
    "s_add_u32 m0, s53, 41984\n\t"
    "v_mfma_f32_16x16x32_bf16 v[112:115], a[36:39], v[56:59], v[112:115]\n\t"
    "buffer_load_dwordx4 v3, s[20:23], s42 offen lds\n\t"
    "v_mfma_f32_16x16x32_bf16 v[112:115], a[48:51], v[60:63], v[112:115]\n\t"
    "v_mfma_f32_16x16x32_bf16 v[112:115], a[60:63], v[32:35], v[112:115]\n\t"
    "v_mfma_f32_16x16x32_bf16 v[116:119], a[40:43], v[56:59], v[116:119]\n\t"
    "v_mfma_f32_16x16x32_bf16 v[116:119], a[52:55], v[60:63], v[116:119]\n\t"
    "v_mfma_f32_16x16x32_bf16 v[116:119], a[64:67], v[32:35], v[116:119]\n\t"
    "v_mfma_f32_16x16x32_bf16 v[120:123], a[44:47], v[56:59], v[120:123]\n\t"
    "v_mfma_f32_16x16x32_bf16 v[120:123], a[56:59], v[60:63], v[120:123]\n\t"
    "v_mfma_f32_16x16x32_bf16 v[120:123], a[68:71], v[32:35], v[120:123]\n\t"
    "s_waitcnt lgkmcnt(2)\n\t"
    "v_mfma_f32_16x16x32_bf16 v[148:151], v[220:223], v[36:39], v[148:151]\n\t"
    "s_waitcnt lgkmcnt(1)\n\t"
    "v_mfma_f32_16x16x32_bf16 v[148:151], v[232:235], v[40:43], v[148:151]\n\t"
    "s_waitcnt lgkmcnt(0)\n\t"
    "v_mfma_f32_16x16x32_bf16 v[148:151], v[244:247], v[44:47], v[148:151]\n\t"
    "v_mfma_f32_16x16x32_bf16 v[152:155], v[224:227], v[36:39], v[152:155]\n\t"
    "v_mfma_f32_16x16x32_bf16 v[152:155], v[236:239], v[40:43], v[152:155]\n\t"
    "v_mfma_f32_16x16x32_bf16 v[152:155], v[248:251], v[44:47], v[152:155]\n\t"
    "v_mfma_f32_16x16x32_bf16 v[156:159], v[228:231], v[36:39], v[156:159]\n\t"
    "v_mfma_f32_16x16x32_bf16 v[156:159], v[240:243], v[40:43], v[156:159]\n\t"
    "v_mfma_f32_16x16x32_bf16 v[156:159], v[252:255], v[44:47], v[156:159]\n\t"
    "v_mfma_f32_16x16x32_bf16 v[76:79], a[180:183], v[36:39], v[76:79]\n\t"
    "v_mfma_f32_16x16x32_bf16 v[76:79], a[192:195], v[40:43], v[76:79]\n\t"
    "v_mfma_f32_16x16x32_bf16 v[76:79], a[204:207], v[44:47], v[76:79]\n\t"
    "v_mfma_f32_16x16x32_bf16 v[80:83], a[184:187], v[36:39], v[80:83]\n\t"
    "ds_read_b128 v[48:51], v13 offset:26624\n\t"
    "v_mfma_f32_16x16x32_bf16 v[80:83], a[196:199], v[40:43], v[80:83]\n\t"
    "ds_read_b128 v[52:55], v13 offset:26688\n\t"
    "v_mfma_f32_16x16x32_bf16 v[80:83], a[208:211], v[44:47], v[80:83]\n\t"
    "ds_read_b128 v[56:59], v13 offset:26752\n\t"
    "v_mfma_f32_16x16x32_bf16 v[84:87], a[188:191], v[36:39], v[84:87]\n\t"
    "v_mfma_f32_16x16x32_bf16 v[84:87], a[200:203], v[40:43], v[84:87]\n\t"
    "s_add_u32 s78, s46, 3072\n\t"
    "v_mfma_f32_16x16x32_bf16 v[84:87], a[212:215], v[44:47], v[84:87]\n\t"
    "v_cvt_pk_bf16_f32 v182, v156, v157\n\t"
    "v_mfma_f32_16x16x32_bf16 v[112:115], a[72:75], v[36:39], v[112:115]\n\t"
    "v_cvt_pk_bf16_f32 v183, v158, v159\n\t"
    "v_mfma_f32_16x16x32_bf16 v[112:115], a[84:87], v[40:43], v[112:115]\n\t"
    "ds_write_b64 v25, v[182:183] offset:3840\n\t"
    "v_mfma_f32_16x16x32_bf16 v[112:115], a[96:99], v[44:47], v[112:115]\n\t"
    "v_cvt_pk_bf16_f32 v178, v148, v149\n\t"
    "v_mfma_f32_16x16x32_bf16 v[116:119], a[76:79], v[36:39], v[116:119]\n\t"
    "v_cvt_pk_bf16_f32 v179, v150, v151\n\t"
    "v_mfma_f32_16x16x32_bf16 v[116:119], a[88:91], v[40:43], v[116:119]\n\t"
    "v_cvt_pk_bf16_f32 v180, v152, v153\n\t"
    "v_mfma_f32_16x16x32_bf16 v[116:119], a[100:103], v[44:47], v[116:119]\n\t"
    "v_cvt_pk_bf16_f32 v181, v154, v155\n\t"
    "v_mfma_f32_16x16x32_bf16 v[120:123], a[80:83], v[36:39], v[120:123]\n\t"
    "buffer_store_dwordx4 v[178:181], v22, s[28:31], s78 offen nt\n\t"
    "v_mfma_f32_16x16x32_bf16 v[120:123], a[92:95], v[40:43], v[120:123]\n\t"
    "v_mfma_f32_16x16x32_bf16 v[120:123], a[104:107], v[44:47], v[120:123]\n\t"
    "s_waitcnt lgkmcnt(3)\n\t"
    "v_mfma_f32_16x16x32_bf16 v[160:163], a[216:219], v[48:51], v[160:163]\n\t"
    "s_waitcnt lgkmcnt(2)\n\t"
    "v_mfma_f32_16x16x32_bf16 v[160:163], a[228:231], v[52:55], v[160:163]\n\t"
    "s_waitcnt lgkmcnt(0)\n\t"
    "s_barrier\n\t"
    "v_mfma_f32_16x16x32_bf16 v[160:163], a[240:243], v[56:59], v[160:163]\n\t"
    "ds_read_b128 v[28:31], v26 offset:2560\n\t"
    "v_mfma_f32_16x16x32_bf16 v[164:167], a[220:223], v[48:51], v[164:167]\n\t"
    "s_add_u32 s96, s46, 0\n\t"
    "s_add_u32 s97, s46, 3072\n\t"
    "s_add_u32 m0, s53, 43008\n\t"
    "v_mfma_f32_16x16x32_bf16 v[164:167], a[232:235], v[52:55], v[164:167]\n\t"
    "s_cmp_eq_u32 s98, 0\n\t"
    "s_cselect_b32 s96, s96, s97\n\t"
    "buffer_load_dwordx4 v4, s[20:23], s42 offen lds\n\t"
    "v_mfma_f32_16x16x32_bf16 v[164:167], a[244:247], v[56:59], v[164:167]\n\t"
    "v_mfma_f32_16x16x32_bf16 v[168:171], a[224:227], v[48:51], v[168:171]\n\t"
    "v_mfma_f32_16x16x32_bf16 v[168:171], a[236:239], v[52:55], v[168:171]\n\t"
    "v_mfma_f32_16x16x32_bf16 v[168:171], a[248:251], v[56:59], v[168:171]\n\t"
    "v_mfma_f32_16x16x32_bf16 v[88:91], a[108:111], v[48:51], v[88:91]\n\t"
    "v_mfma_f32_16x16x32_bf16 v[88:91], a[120:123], v[52:55], v[88:91]\n\t"
    "v_mfma_f32_16x16x32_bf16 v[88:91], a[132:135], v[56:59], v[88:91]\n\t"
    "v_mfma_f32_16x16x32_bf16 v[92:95], a[112:115], v[48:51], v[92:95]\n\t"
    "ds_read_b128 v[60:63], v14 offset:26624\n\t"
    "v_mfma_f32_16x16x32_bf16 v[92:95], a[124:127], v[52:55], v[92:95]\n\t"
    "ds_read_b128 v[32:35], v14 offset:26688\n\t"
    "v_mfma_f32_16x16x32_bf16 v[92:95], a[136:139], v[56:59], v[92:95]\n\t"
    "s_waitcnt lgkmcnt(2)\n\t"
    "buffer_store_dwordx4 v[28:31], v23, s[28:31], s96 offen nt\n\t"
    "ds_read_b128 v[36:39], v14 offset:26752\n\t"
    "v_mfma_f32_16x16x32_bf16 v[96:99], a[116:119], v[48:51], v[96:99]\n\t"
    "v_mfma_f32_16x16x32_bf16 v[96:99], a[128:131], v[52:55], v[96:99]\n\t"
    "v_mfma_f32_16x16x32_bf16 v[96:99], a[140:143], v[56:59], v[96:99]\n\t"
    "v_mfma_f32_16x16x32_bf16 v[124:127], a[0:3], v[48:51], 0\n\t"
    "v_mfma_f32_16x16x32_bf16 v[124:127], a[12:15], v[52:55], v[124:127]\n\t"
    "v_mfma_f32_16x16x32_bf16 v[124:127], a[24:27], v[56:59], v[124:127]\n\t"
    "v_mfma_f32_16x16x32_bf16 v[128:131], a[4:7], v[48:51], 0\n\t"
    "v_mfma_f32_16x16x32_bf16 v[128:131], a[16:19], v[52:55], v[128:131]\n\t"
    "v_mfma_f32_16x16x32_bf16 v[128:131], a[28:31], v[56:59], v[128:131]\n\t"
    "v_mfma_f32_16x16x32_bf16 v[132:135], a[8:11], v[48:51], 0\n\t"
    "v_mfma_f32_16x16x32_bf16 v[132:135], a[20:23], v[52:55], v[132:135]\n\t"
    "v_mfma_f32_16x16x32_bf16 v[132:135], a[32:35], v[56:59], v[132:135]\n\t"
    "s_waitcnt lgkmcnt(2)\n\t"
    "v_mfma_f32_16x16x32_bf16 v[160:163], a[252:255], v[60:63], v[160:163]\n\t"
    "s_waitcnt lgkmcnt(1)\n\t"
    "v_mfma_f32_16x16x32_bf16 v[160:163], v[196:199], v[32:35], v[160:163]\n\t"
    "s_waitcnt lgkmcnt(0)\n\t"
    "v_mfma_f32_16x16x32_bf16 v[160:163], v[208:211], v[36:39], v[160:163]\n\t"
    "v_mfma_f32_16x16x32_bf16 v[164:167], v[188:191], v[60:63], v[164:167]\n\t"
    "v_mfma_f32_16x16x32_bf16 v[164:167], v[200:203], v[32:35], v[164:167]\n\t"
    "v_mfma_f32_16x16x32_bf16 v[164:167], v[212:215], v[36:39], v[164:167]\n\t"
    "v_mfma_f32_16x16x32_bf16 v[168:171], v[192:195], v[60:63], v[168:171]\n\t"
    "v_mfma_f32_16x16x32_bf16 v[168:171], v[204:207], v[32:35], v[168:171]\n\t"
    "v_mfma_f32_16x16x32_bf16 v[168:171], v[216:219], v[36:39], v[168:171]\n\t"
    "v_mfma_f32_16x16x32_bf16 v[88:91], a[144:147], v[60:63], v[88:91]\n\t"
    "v_mfma_f32_16x16x32_bf16 v[88:91], a[156:159], v[32:35], v[88:91]\n\t"
    "v_mfma_f32_16x16x32_bf16 v[88:91], a[168:171], v[36:39], v[88:91]\n\t"
    "v_mfma_f32_16x16x32_bf16 v[92:95], a[148:151], v[60:63], v[92:95]\n\t"
    "ds_read_b128 v[40:43], v15 offset:26624\n\t"
    "v_mfma_f32_16x16x32_bf16 v[92:95], a[160:163], v[32:35], v[92:95]\n\t"
    "ds_read_b128 v[44:47], v15 offset:26688\n\t"
    "v_mfma_f32_16x16x32_bf16 v[92:95], a[172:175], v[36:39], v[92:95]\n\t"
    "ds_read_b128 v[48:51], v15 offset:26752\n\t"
    "v_mfma_f32_16x16x32_bf16 v[96:99], a[152:155], v[60:63], v[96:99]\n\t"
    "v_mfma_f32_16x16x32_bf16 v[96:99], a[164:167], v[32:35], v[96:99]\n\t"
    "s_add_u32 m0, s53, 44032\n\t"
    "v_mfma_f32_16x16x32_bf16 v[96:99], a[176:179], v[36:39], v[96:99]\n\t"
    "buffer_load_dwordx4 v5, s[20:23], s42 offen lds\n\t"
    "v_mfma_f32_16x16x32_bf16 v[124:127], a[36:39], v[60:63], v[124:127]\n\t"
    "v_mfma_f32_16x16x32_bf16 v[124:127], a[48:51], v[32:35], v[124:127]\n\t"
    "v_mfma_f32_16x16x32_bf16 v[124:127], a[60:63], v[36:39], v[124:127]\n\t"
    "v_mfma_f32_16x16x32_bf16 v[128:131], a[40:43], v[60:63], v[128:131]\n\t"
    "v_mfma_f32_16x16x32_bf16 v[128:131], a[52:55], v[32:35], v[128:131]\n\t"
    "v_mfma_f32_16x16x32_bf16 v[128:131], a[64:67], v[36:39], v[128:131]\n\t"
    "v_mfma_f32_16x16x32_bf16 v[132:135], a[44:47], v[60:63], v[132:135]\n\t"
    "v_mfma_f32_16x16x32_bf16 v[132:135], a[56:59], v[32:35], v[132:135]\n\t"
    "v_mfma_f32_16x16x32_bf16 v[132:135], a[68:71], v[36:39], v[132:135]\n\t"
    "s_waitcnt lgkmcnt(2)\n\t"
    "v_mfma_f32_16x16x32_bf16 v[160:163], v[220:223], v[40:43], v[160:163]\n\t"
    "s_waitcnt lgkmcnt(1)\n\t"
    "v_mfma_f32_16x16x32_bf16 v[160:163], v[232:235], v[44:47], v[160:163]\n\t"
    "s_waitcnt lgkmcnt(0)\n\t"
    "v_mfma_f32_16x16x32_bf16 v[160:163], v[244:247], v[48:51], v[160:163]\n\t"
    "v_mfma_f32_16x16x32_bf16 v[164:167], v[224:227], v[40:43], v[164:167]\n\t"
    "v_mfma_f32_16x16x32_bf16 v[164:167], v[236:239], v[44:47], v[164:167]\n\t"
    "v_mfma_f32_16x16x32_bf16 v[164:167], v[248:251], v[48:51], v[164:167]\n\t"
    "v_mfma_f32_16x16x32_bf16 v[168:171], v[228:231], v[40:43], v[168:171]\n\t"
    "v_mfma_f32_16x16x32_bf16 v[168:171], v[240:243], v[44:47], v[168:171]\n\t"
    "v_mfma_f32_16x16x32_bf16 v[168:171], v[252:255], v[48:51], v[168:171]\n\t"
    "v_mfma_f32_16x16x32_bf16 v[88:91], a[180:183], v[40:43], v[88:91]\n\t"
    "v_mfma_f32_16x16x32_bf16 v[88:91], a[192:195], v[44:47], v[88:91]\n\t"
    "s_waitcnt vmcnt(8)\n\t"
    "s_barrier\n\t"
    "v_mfma_f32_16x16x32_bf16 v[88:91], a[204:207], v[48:51], v[88:91]\n\t"
    "v_mfma_f32_16x16x32_bf16 v[92:95], a[184:187], v[40:43], v[92:95]\n\t"
    "ds_read_b128 v[52:55], v7 offset:20480\n\t"
    "v_mfma_f32_16x16x32_bf16 v[92:95], a[196:199], v[44:47], v[92:95]\n\t"
    "ds_read_b128 v[56:59], v7 offset:20544\n\t"
    "v_mfma_f32_16x16x32_bf16 v[92:95], a[208:211], v[48:51], v[92:95]\n\t"
    "ds_read_b128 v[60:63], v7 offset:20608\n\t"
    "v_mfma_f32_16x16x32_bf16 v[96:99], a[188:191], v[40:43], v[96:99]\n\t"
    "v_mfma_f32_16x16x32_bf16 v[96:99], a[200:203], v[44:47], v[96:99]\n\t"
    "s_add_u32 s77, s46, 6144\n\t"
    "v_mfma_f32_16x16x32_bf16 v[96:99], a[212:215], v[48:51], v[96:99]\n\t"
    "v_cvt_pk_bf16_f32 v176, v168, v169\n\t"
    "v_mfma_f32_16x16x32_bf16 v[124:127], a[72:75], v[40:43], v[124:127]\n\t"
    "v_cvt_pk_bf16_f32 v177, v170, v171\n\t"
    "v_mfma_f32_16x16x32_bf16 v[124:127], a[84:87], v[44:47], v[124:127]\n\t"
    "ds_write_b64 v25, v[176:177] offset:0\n\t"
    "v_mfma_f32_16x16x32_bf16 v[124:127], a[96:99], v[48:51], v[124:127]\n\t"
    "v_cvt_pk_bf16_f32 v172, v160, v161\n\t"
    "v_mfma_f32_16x16x32_bf16 v[128:131], a[76:79], v[40:43], v[128:131]\n\t"
    "v_cvt_pk_bf16_f32 v173, v162, v163\n\t"
    "v_mfma_f32_16x16x32_bf16 v[128:131], a[88:91], v[44:47], v[128:131]\n\t"
    "v_cvt_pk_bf16_f32 v174, v164, v165\n\t"
    "v_mfma_f32_16x16x32_bf16 v[128:131], a[100:103], v[48:51], v[128:131]\n\t"
    "v_cvt_pk_bf16_f32 v175, v166, v167\n\t"
    "v_mfma_f32_16x16x32_bf16 v[132:135], a[80:83], v[40:43], v[132:135]\n\t"
    "buffer_store_dwordx4 v[172:175], v22, s[28:31], s77 offen nt\n\t"
    "v_mfma_f32_16x16x32_bf16 v[132:135], a[92:95], v[44:47], v[132:135]\n\t"
    "v_mfma_f32_16x16x32_bf16 v[132:135], a[104:107], v[48:51], v[132:135]\n\t"
    "s_waitcnt lgkmcnt(3)\n\t"
    "v_mfma_f32_16x16x32_bf16 v[64:67], a[216:219], v[52:55], v[64:67]\n\t"
    "s_waitcnt lgkmcnt(2)\n\t"
    "v_mfma_f32_16x16x32_bf16 v[64:67], a[228:231], v[56:59], v[64:67]\n\t"
    "s_waitcnt lgkmcnt(1)\n\t"
    "v_mfma_f32_16x16x32_bf16 v[64:67], a[240:243], v[60:63], v[64:67]\n\t"
    "s_add_u32 m0, s53, 45056\n\t"
    "v_mfma_f32_16x16x32_bf16 v[68:71], a[220:223], v[52:55], v[68:71]\n\t"
    "buffer_load_dwordx4 v6, s[20:23], s42 offen lds\n\t"
    "v_mfma_f32_16x16x32_bf16 v[68:71], a[232:235], v[56:59], v[68:71]\n\t"
    "s_add_u32 m0, s53, 61440\n\t"
    "v_mfma_f32_16x16x32_bf16 v[68:71], a[244:247], v[60:63], v[68:71]\n\t"
    "buffer_load_dwordx4 v2, s[20:23], s43 offen lds\n\t"
    "v_mfma_f32_16x16x32_bf16 v[72:75], a[224:227], v[52:55], v[72:75]\n\t"
    "v_mfma_f32_16x16x32_bf16 v[72:75], a[236:239], v[56:59], v[72:75]\n\t"
    "v_mfma_f32_16x16x32_bf16 v[72:75], a[248:251], v[60:63], v[72:75]\n\t"
    "v_mfma_f32_16x16x32_bf16 v[100:103], a[108:111], v[52:55], v[100:103]\n\t"
    "v_mfma_f32_16x16x32_bf16 v[100:103], a[120:123], v[56:59], v[100:103]\n\t"
    "v_mfma_f32_16x16x32_bf16 v[100:103], a[132:135], v[60:63], v[100:103]\n\t"
    "v_mfma_f32_16x16x32_bf16 v[104:107], a[112:115], v[52:55], v[104:107]\n\t"
    "ds_read_b128 v[32:35], v8 offset:20480\n\t"
    "v_mfma_f32_16x16x32_bf16 v[104:107], a[124:127], v[56:59], v[104:107]\n\t"
    "ds_read_b128 v[36:39], v8 offset:20544\n\t"
    "v_mfma_f32_16x16x32_bf16 v[104:107], a[136:139], v[60:63], v[104:107]\n\t"
    "ds_read_b128 v[40:43], v8 offset:20608\n\t"
    "v_mfma_f32_16x16x32_bf16 v[108:111], a[116:119], v[52:55], v[108:111]\n\t"
    "v_mfma_f32_16x16x32_bf16 v[108:111], a[128:131], v[56:59], v[108:111]\n\t"
    "v_mfma_f32_16x16x32_bf16 v[108:111], a[140:143], v[60:63], v[108:111]\n\t"
    "v_mfma_f32_16x16x32_bf16 v[136:139], a[0:3], v[52:55], 0\n\t"
    "v_mfma_f32_16x16x32_bf16 v[136:139], a[12:15], v[56:59], v[136:139]\n\t"
    "v_mfma_f32_16x16x32_bf16 v[136:139], a[24:27], v[60:63], v[136:139]\n\t"
    "v_mfma_f32_16x16x32_bf16 v[140:143], a[4:7], v[52:55], 0\n\t"
    "v_mfma_f32_16x16x32_bf16 v[140:143], a[16:19], v[56:59], v[140:143]\n\t"
    "v_mfma_f32_16x16x32_bf16 v[140:143], a[28:31], v[60:63], v[140:143]\n\t"
    "v_mfma_f32_16x16x32_bf16 v[144:147], a[8:11], v[52:55], 0\n\t"
    "v_mfma_f32_16x16x32_bf16 v[144:147], a[20:23], v[56:59], v[144:147]\n\t"
    "v_mfma_f32_16x16x32_bf16 v[144:147], a[32:35], v[60:63], v[144:147]\n\t"
    "s_waitcnt lgkmcnt(2)\n\t"
    "v_mfma_f32_16x16x32_bf16 v[64:67], a[252:255], v[32:35], v[64:67]\n\t"
    "s_waitcnt lgkmcnt(1)\n\t"
    "v_mfma_f32_16x16x32_bf16 v[64:67], v[196:199], v[36:39], v[64:67]\n\t"
    "s_waitcnt lgkmcnt(0)\n\t"
    "v_mfma_f32_16x16x32_bf16 v[64:67], v[208:211], v[40:43], v[64:67]\n\t"
    "v_mfma_f32_16x16x32_bf16 v[68:71], v[188:191], v[32:35], v[68:71]\n\t"
    "v_mfma_f32_16x16x32_bf16 v[68:71], v[200:203], v[36:39], v[68:71]\n\t"
    "v_mfma_f32_16x16x32_bf16 v[68:71], v[212:215], v[40:43], v[68:71]\n\t"
    "v_mfma_f32_16x16x32_bf16 v[72:75], v[192:195], v[32:35], v[72:75]\n\t"
    "v_mfma_f32_16x16x32_bf16 v[72:75], v[204:207], v[36:39], v[72:75]\n\t"
    "v_mfma_f32_16x16x32_bf16 v[72:75], v[216:219], v[40:43], v[72:75]\n\t"
    "v_mfma_f32_16x16x32_bf16 v[100:103], a[144:147], v[32:35], v[100:103]\n\t"
    "v_mfma_f32_16x16x32_bf16 v[100:103], a[156:159], v[36:39], v[100:103]\n\t"
    "v_mfma_f32_16x16x32_bf16 v[100:103], a[168:171], v[40:43], v[100:103]\n\t"
    "v_mfma_f32_16x16x32_bf16 v[104:107], a[148:151], v[32:35], v[104:107]\n\t"
    "ds_read_b128 v[44:47], v9 offset:20480\n\t"
    "v_mfma_f32_16x16x32_bf16 v[104:107], a[160:163], v[36:39], v[104:107]\n\t"
    "ds_read_b128 v[48:51], v9 offset:20544\n\t"
    "v_mfma_f32_16x16x32_bf16 v[104:107], a[172:175], v[40:43], v[104:107]\n\t"
    "ds_read_b128 v[52:55], v9 offset:20608\n\t"
    "v_mfma_f32_16x16x32_bf16 v[108:111], a[152:155], v[32:35], v[108:111]\n\t"
    "v_mfma_f32_16x16x32_bf16 v[108:111], a[164:167], v[36:39], v[108:111]\n\t"
    "v_mfma_f32_16x16x32_bf16 v[108:111], a[176:179], v[40:43], v[108:111]\n\t"
    "s_add_u32 m0, s53, 62464\n\t"
    "v_mfma_f32_16x16x32_bf16 v[136:139], a[36:39], v[32:35], v[136:139]\n\t"
    "buffer_load_dwordx4 v3, s[20:23], s43 offen lds\n\t"
    "v_mfma_f32_16x16x32_bf16 v[136:139], a[48:51], v[36:39], v[136:139]\n\t"
    "v_mfma_f32_16x16x32_bf16 v[136:139], a[60:63], v[40:43], v[136:139]\n\t"
    "v_mfma_f32_16x16x32_bf16 v[140:143], a[40:43], v[32:35], v[140:143]\n\t"
    "v_mfma_f32_16x16x32_bf16 v[140:143], a[52:55], v[36:39], v[140:143]\n\t"
    "v_mfma_f32_16x16x32_bf16 v[140:143], a[64:67], v[40:43], v[140:143]\n\t"
    "v_mfma_f32_16x16x32_bf16 v[144:147], a[44:47], v[32:35], v[144:147]\n\t"
    "v_mfma_f32_16x16x32_bf16 v[144:147], a[56:59], v[36:39], v[144:147]\n\t"
    "v_mfma_f32_16x16x32_bf16 v[144:147], a[68:71], v[40:43], v[144:147]\n\t"
    "s_waitcnt lgkmcnt(2)\n\t"
    "v_mfma_f32_16x16x32_bf16 v[64:67], v[220:223], v[44:47], v[64:67]\n\t"
    "s_waitcnt lgkmcnt(1)\n\t"
    "v_mfma_f32_16x16x32_bf16 v[64:67], v[232:235], v[48:51], v[64:67]\n\t"
    "s_waitcnt lgkmcnt(0)\n\t"
    "v_mfma_f32_16x16x32_bf16 v[64:67], v[244:247], v[52:55], v[64:67]\n\t"
    "v_mfma_f32_16x16x32_bf16 v[68:71], v[224:227], v[44:47], v[68:71]\n\t"
    "v_mfma_f32_16x16x32_bf16 v[68:71], v[236:239], v[48:51], v[68:71]\n\t"
    "v_mfma_f32_16x16x32_bf16 v[68:71], v[248:251], v[52:55], v[68:71]\n\t"
    "v_mfma_f32_16x16x32_bf16 v[72:75], v[228:231], v[44:47], v[72:75]\n\t"
    "v_mfma_f32_16x16x32_bf16 v[72:75], v[240:243], v[48:51], v[72:75]\n\t"
    "v_mfma_f32_16x16x32_bf16 v[72:75], v[252:255], v[52:55], v[72:75]\n\t"
    "v_mfma_f32_16x16x32_bf16 v[100:103], a[180:183], v[44:47], v[100:103]\n\t"
    "v_mfma_f32_16x16x32_bf16 v[100:103], a[192:195], v[48:51], v[100:103]\n\t"
    "v_mfma_f32_16x16x32_bf16 v[100:103], a[204:207], v[52:55], v[100:103]\n\t"
    "v_mfma_f32_16x16x32_bf16 v[104:107], a[184:187], v[44:47], v[104:107]\n\t"
    "ds_read_b128 v[56:59], v7 offset:23552\n\t"
    "v_mfma_f32_16x16x32_bf16 v[104:107], a[196:199], v[48:51], v[104:107]\n\t"
    "ds_read_b128 v[60:63], v7 offset:23616\n\t"
    "v_mfma_f32_16x16x32_bf16 v[104:107], a[208:211], v[52:55], v[104:107]\n\t"
    "ds_read_b128 v[32:35], v7 offset:23680\n\t"
    "v_mfma_f32_16x16x32_bf16 v[108:111], a[188:191], v[44:47], v[108:111]\n\t"
    "v_mfma_f32_16x16x32_bf16 v[108:111], a[200:203], v[48:51], v[108:111]\n\t"
    "s_add_u32 s78, s47, 0\n\t"
    "v_mfma_f32_16x16x32_bf16 v[108:111], a[212:215], v[52:55], v[108:111]\n\t"
    "v_cvt_pk_bf16_f32 v182, v72, v73\n\t"
    "v_mfma_f32_16x16x32_bf16 v[136:139], a[72:75], v[44:47], v[136:139]\n\t"
    "v_cvt_pk_bf16_f32 v183, v74, v75\n\t"
    "v_mfma_f32_16x16x32_bf16 v[136:139], a[84:87], v[48:51], v[136:139]\n\t"
    "ds_write_b64 v25, v[182:183] offset:1280\n\t"
    "v_mfma_f32_16x16x32_bf16 v[136:139], a[96:99], v[52:55], v[136:139]\n\t"
    "v_cvt_pk_bf16_f32 v178, v64, v65\n\t"
    "v_mfma_f32_16x16x32_bf16 v[140:143], a[76:79], v[44:47], v[140:143]\n\t"
    "v_cvt_pk_bf16_f32 v179, v66, v67\n\t"
    "v_mfma_f32_16x16x32_bf16 v[140:143], a[88:91], v[48:51], v[140:143]\n\t"
    "v_cvt_pk_bf16_f32 v180, v68, v69\n\t"
    "v_mfma_f32_16x16x32_bf16 v[140:143], a[100:103], v[52:55], v[140:143]\n\t"
    "v_cvt_pk_bf16_f32 v181, v70, v71\n\t"
    "v_mfma_f32_16x16x32_bf16 v[144:147], a[80:83], v[44:47], v[144:147]\n\t"
    "buffer_store_dwordx4 v[178:181], v22, s[28:31], s78 offen nt\n\t"
    "v_mfma_f32_16x16x32_bf16 v[144:147], a[92:95], v[48:51], v[144:147]\n\t"
    "v_mfma_f32_16x16x32_bf16 v[144:147], a[104:107], v[52:55], v[144:147]\n\t"
    "s_waitcnt lgkmcnt(3)\n\t"
    "v_mfma_f32_16x16x32_bf16 v[76:79], a[216:219], v[56:59], v[76:79]\n\t"
    "s_waitcnt lgkmcnt(2)\n\t"
    "v_mfma_f32_16x16x32_bf16 v[76:79], a[228:231], v[60:63], v[76:79]\n\t"
    "s_waitcnt lgkmcnt(0)\n\t"
    "s_barrier\n\t"
    "v_mfma_f32_16x16x32_bf16 v[76:79], a[240:243], v[32:35], v[76:79]\n\t"
    "ds_read_b128 v[28:31], v26 offset:0\n\t"
    "v_mfma_f32_16x16x32_bf16 v[80:83], a[220:223], v[56:59], v[80:83]\n\t"
    "s_add_u32 s96, s46, 6144\n\t"
    "s_add_u32 s97, s47, 0\n\t"
    "s_add_u32 m0, s53, 63488\n\t"
    "v_mfma_f32_16x16x32_bf16 v[80:83], a[232:235], v[60:63], v[80:83]\n\t"
    "s_cmp_eq_u32 s98, 0\n\t"
    "s_cselect_b32 s96, s96, s97\n\t"
    "buffer_load_dwordx4 v4, s[20:23], s43 offen lds\n\t"
    "v_mfma_f32_16x16x32_bf16 v[80:83], a[244:247], v[32:35], v[80:83]\n\t"
    "v_mfma_f32_16x16x32_bf16 v[84:87], a[224:227], v[56:59], v[84:87]\n\t"
    "v_mfma_f32_16x16x32_bf16 v[84:87], a[236:239], v[60:63], v[84:87]\n\t"
    "v_mfma_f32_16x16x32_bf16 v[84:87], a[248:251], v[32:35], v[84:87]\n\t"
    "v_mfma_f32_16x16x32_bf16 v[112:115], a[108:111], v[56:59], v[112:115]\n\t"
    "v_mfma_f32_16x16x32_bf16 v[112:115], a[120:123], v[60:63], v[112:115]\n\t"
    "v_mfma_f32_16x16x32_bf16 v[112:115], a[132:135], v[32:35], v[112:115]\n\t"
    "v_mfma_f32_16x16x32_bf16 v[116:119], a[112:115], v[56:59], v[116:119]\n\t"
    "ds_read_b128 v[36:39], v8 offset:23552\n\t"
    "v_mfma_f32_16x16x32_bf16 v[116:119], a[124:127], v[60:63], v[116:119]\n\t"
    "ds_read_b128 v[40:43], v8 offset:23616\n\t"
    "v_mfma_f32_16x16x32_bf16 v[116:119], a[136:139], v[32:35], v[116:119]\n\t"
    "s_waitcnt lgkmcnt(2)\n\t"
    "buffer_store_dwordx4 v[28:31], v23, s[28:31], s96 offen nt\n\t"
    "ds_read_b128 v[44:47], v8 offset:23680\n\t"
    "v_mfma_f32_16x16x32_bf16 v[120:123], a[116:119], v[56:59], v[120:123]\n\t"
    "v_mfma_f32_16x16x32_bf16 v[120:123], a[128:131], v[60:63], v[120:123]\n\t"
    "v_mfma_f32_16x16x32_bf16 v[120:123], a[140:143], v[32:35], v[120:123]\n\t"
    "v_mfma_f32_16x16x32_bf16 v[148:151], a[0:3], v[56:59], 0\n\t"
    "v_mfma_f32_16x16x32_bf16 v[148:151], a[12:15], v[60:63], v[148:151]\n\t"
    "v_mfma_f32_16x16x32_bf16 v[148:151], a[24:27], v[32:35], v[148:151]\n\t"
    "v_mfma_f32_16x16x32_bf16 v[152:155], a[4:7], v[56:59], 0\n\t"
    "v_mfma_f32_16x16x32_bf16 v[152:155], a[16:19], v[60:63], v[152:155]\n\t"
    "v_mfma_f32_16x16x32_bf16 v[152:155], a[28:31], v[32:35], v[152:155]\n\t"
    "v_mfma_f32_16x16x32_bf16 v[156:159], a[8:11], v[56:59], 0\n\t"
    "v_mfma_f32_16x16x32_bf16 v[156:159], a[20:23], v[60:63], v[156:159]\n\t"
    "v_mfma_f32_16x16x32_bf16 v[156:159], a[32:35], v[32:35], v[156:159]\n\t"
    "s_waitcnt lgkmcnt(2)\n\t"
    "v_mfma_f32_16x16x32_bf16 v[76:79], a[252:255], v[36:39], v[76:79]\n\t"
    "s_waitcnt lgkmcnt(1)\n\t"
    "v_mfma_f32_16x16x32_bf16 v[76:79], v[196:199], v[40:43], v[76:79]\n\t"
    "s_waitcnt lgkmcnt(0)\n\t"
    "v_mfma_f32_16x16x32_bf16 v[76:79], v[208:211], v[44:47], v[76:79]\n\t"
    "v_mfma_f32_16x16x32_bf16 v[80:83], v[188:191], v[36:39], v[80:83]\n\t"
    "v_mfma_f32_16x16x32_bf16 v[80:83], v[200:203], v[40:43], v[80:83]\n\t"
    "v_mfma_f32_16x16x32_bf16 v[80:83], v[212:215], v[44:47], v[80:83]\n\t"
    "v_mfma_f32_16x16x32_bf16 v[84:87], v[192:195], v[36:39], v[84:87]\n\t"
    "v_mfma_f32_16x16x32_bf16 v[84:87], v[204:207], v[40:43], v[84:87]\n\t"
    "v_mfma_f32_16x16x32_bf16 v[84:87], v[216:219], v[44:47], v[84:87]\n\t"
    "v_mfma_f32_16x16x32_bf16 v[112:115], a[144:147], v[36:39], v[112:115]\n\t"
    "v_mfma_f32_16x16x32_bf16 v[112:115], a[156:159], v[40:43], v[112:115]\n\t"
    "v_mfma_f32_16x16x32_bf16 v[112:115], a[168:171], v[44:47], v[112:115]\n\t"
    "v_mfma_f32_16x16x32_bf16 v[116:119], a[148:151], v[36:39], v[116:119]\n\t"
    "ds_read_b128 v[48:51], v9 offset:23552\n\t"
    "v_mfma_f32_16x16x32_bf16 v[116:119], a[160:163], v[40:43], v[116:119]\n\t"
    "ds_read_b128 v[52:55], v9 offset:23616\n\t"
    "v_mfma_f32_16x16x32_bf16 v[116:119], a[172:175], v[44:47], v[116:119]\n\t"
    "ds_read_b128 v[56:59], v9 offset:23680\n\t"
    "v_mfma_f32_16x16x32_bf16 v[120:123], a[152:155], v[36:39], v[120:123]\n\t"
    "v_mfma_f32_16x16x32_bf16 v[120:123], a[164:167], v[40:43], v[120:123]\n\t"
    "s_add_u32 m0, s53, 64512\n\t"
    "v_mfma_f32_16x16x32_bf16 v[120:123], a[176:179], v[44:47], v[120:123]\n\t"
    "buffer_load_dwordx4 v5, s[20:23], s43 offen lds\n\t"
    "v_mfma_f32_16x16x32_bf16 v[148:151], a[36:39], v[36:39], v[148:151]\n\t"
    "v_mfma_f32_16x16x32_bf16 v[148:151], a[48:51], v[40:43], v[148:151]\n\t"
    "v_mfma_f32_16x16x32_bf16 v[148:151], a[60:63], v[44:47], v[148:151]\n\t"
    "v_mfma_f32_16x16x32_bf16 v[152:155], a[40:43], v[36:39], v[152:155]\n\t"
    "v_mfma_f32_16x16x32_bf16 v[152:155], a[52:55], v[40:43], v[152:155]\n\t"
    "v_mfma_f32_16x16x32_bf16 v[152:155], a[64:67], v[44:47], v[152:155]\n\t"
    "v_mfma_f32_16x16x32_bf16 v[156:159], a[44:47], v[36:39], v[156:159]\n\t"
    "v_mfma_f32_16x16x32_bf16 v[156:159], a[56:59], v[40:43], v[156:159]\n\t"
    "v_mfma_f32_16x16x32_bf16 v[156:159], a[68:71], v[44:47], v[156:159]\n\t"
    "s_waitcnt lgkmcnt(2)\n\t"
    "v_mfma_f32_16x16x32_bf16 v[76:79], v[220:223], v[48:51], v[76:79]\n\t"
    "s_waitcnt lgkmcnt(1)\n\t"
    "v_mfma_f32_16x16x32_bf16 v[76:79], v[232:235], v[52:55], v[76:79]\n\t"
    "s_waitcnt lgkmcnt(0)\n\t"
    "v_mfma_f32_16x16x32_bf16 v[76:79], v[244:247], v[56:59], v[76:79]\n\t"
    "v_mfma_f32_16x16x32_bf16 v[80:83], v[224:227], v[48:51], v[80:83]\n\t"
    "v_mfma_f32_16x16x32_bf16 v[80:83], v[236:239], v[52:55], v[80:83]\n\t"
    "v_mfma_f32_16x16x32_bf16 v[80:83], v[248:251], v[56:59], v[80:83]\n\t"
    "v_mfma_f32_16x16x32_bf16 v[84:87], v[228:231], v[48:51], v[84:87]\n\t"
    "v_mfma_f32_16x16x32_bf16 v[84:87], v[240:243], v[52:55], v[84:87]\n\t"
    "v_mfma_f32_16x16x32_bf16 v[84:87], v[252:255], v[56:59], v[84:87]\n\t"
    "v_mfma_f32_16x16x32_bf16 v[112:115], a[180:183], v[48:51], v[112:115]\n\t"
    "v_mfma_f32_16x16x32_bf16 v[112:115], a[192:195], v[52:55], v[112:115]\n\t"
    "v_mfma_f32_16x16x32_bf16 v[112:115], a[204:207], v[56:59], v[112:115]\n\t"
    "v_mfma_f32_16x16x32_bf16 v[116:119], a[184:187], v[48:51], v[116:119]\n\t"
    "ds_read_b128 v[60:63], v7 offset:26624\n\t"
    "v_mfma_f32_16x16x32_bf16 v[116:119], a[196:199], v[52:55], v[116:119]\n\t"
    "ds_read_b128 v[32:35], v7 offset:26688\n\t"
    "v_mfma_f32_16x16x32_bf16 v[116:119], a[208:211], v[56:59], v[116:119]\n\t"
    "ds_read_b128 v[36:39], v7 offset:26752\n\t"
    "v_mfma_f32_16x16x32_bf16 v[120:123], a[188:191], v[48:51], v[120:123]\n\t"
    "v_mfma_f32_16x16x32_bf16 v[120:123], a[200:203], v[52:55], v[120:123]\n\t"
    "s_add_u32 s77, s47, 3072\n\t"
    "v_mfma_f32_16x16x32_bf16 v[120:123], a[212:215], v[56:59], v[120:123]\n\t"
    "v_cvt_pk_bf16_f32 v176, v84, v85\n\t"
    "v_mfma_f32_16x16x32_bf16 v[148:151], a[72:75], v[48:51], v[148:151]\n\t"
    "v_cvt_pk_bf16_f32 v177, v86, v87\n\t"
    "v_mfma_f32_16x16x32_bf16 v[148:151], a[84:87], v[52:55], v[148:151]\n\t"
    "ds_write_b64 v25, v[176:177] offset:2560\n\t"
    "v_mfma_f32_16x16x32_bf16 v[148:151], a[96:99], v[56:59], v[148:151]\n\t"
    "v_cvt_pk_bf16_f32 v172, v76, v77\n\t"
    "v_mfma_f32_16x16x32_bf16 v[152:155], a[76:79], v[48:51], v[152:155]\n\t"
    "v_cvt_pk_bf16_f32 v173, v78, v79\n\t"
    "v_mfma_f32_16x16x32_bf16 v[152:155], a[88:91], v[52:55], v[152:155]\n\t"
    "v_cvt_pk_bf16_f32 v174, v80, v81\n\t"
    "v_mfma_f32_16x16x32_bf16 v[152:155], a[100:103], v[56:59], v[152:155]\n\t"
    "v_cvt_pk_bf16_f32 v175, v82, v83\n\t"
    "v_mfma_f32_16x16x32_bf16 v[156:159], a[80:83], v[48:51], v[156:159]\n\t"
    "buffer_store_dwordx4 v[172:175], v22, s[28:31], s77 offen nt\n\t"
    "v_mfma_f32_16x16x32_bf16 v[156:159], a[92:95], v[52:55], v[156:159]\n\t"
    "v_mfma_f32_16x16x32_bf16 v[156:159], a[104:107], v[56:59], v[156:159]\n\t"
    "s_waitcnt lgkmcnt(3)\n\t"
    "v_mfma_f32_16x16x32_bf16 v[88:91], a[216:219], v[60:63], v[88:91]\n\t"
    "s_waitcnt lgkmcnt(2)\n\t"
    "v_mfma_f32_16x16x32_bf16 v[88:91], a[228:231], v[32:35], v[88:91]\n\t"
    "s_waitcnt lgkmcnt(1)\n\t"
    "v_mfma_f32_16x16x32_bf16 v[88:91], a[240:243], v[36:39], v[88:91]\n\t"
    "s_add_u32 m0, s53, 65536\n\t"
    "v_mfma_f32_16x16x32_bf16 v[92:95], a[220:223], v[60:63], v[92:95]\n\t"
    "buffer_load_dwordx4 v6, s[20:23], s43 offen lds\n\t"
    "v_mfma_f32_16x16x32_bf16 v[92:95], a[232:235], v[32:35], v[92:95]\n\t"
    "v_mfma_f32_16x16x32_bf16 v[92:95], a[244:247], v[36:39], v[92:95]\n\t"
    "v_mfma_f32_16x16x32_bf16 v[96:99], a[224:227], v[60:63], v[96:99]\n\t"
    "v_mfma_f32_16x16x32_bf16 v[96:99], a[236:239], v[32:35], v[96:99]\n\t"
    "v_mfma_f32_16x16x32_bf16 v[96:99], a[248:251], v[36:39], v[96:99]\n\t"
    "v_mfma_f32_16x16x32_bf16 v[124:127], a[108:111], v[60:63], v[124:127]\n\t"
    "v_mfma_f32_16x16x32_bf16 v[124:127], a[120:123], v[32:35], v[124:127]\n\t"
    "v_mfma_f32_16x16x32_bf16 v[124:127], a[132:135], v[36:39], v[124:127]\n\t"
    "v_mfma_f32_16x16x32_bf16 v[128:131], a[112:115], v[60:63], v[128:131]\n\t"
    "ds_read_b128 v[40:43], v8 offset:26624\n\t"
    "v_mfma_f32_16x16x32_bf16 v[128:131], a[124:127], v[32:35], v[128:131]\n\t"
    "ds_read_b128 v[44:47], v8 offset:26688\n\t"
    "v_mfma_f32_16x16x32_bf16 v[128:131], a[136:139], v[36:39], v[128:131]\n\t"
    "ds_read_b128 v[48:51], v8 offset:26752\n\t"
    "v_mfma_f32_16x16x32_bf16 v[132:135], a[116:119], v[60:63], v[132:135]\n\t"
    "v_mfma_f32_16x16x32_bf16 v[132:135], a[128:131], v[32:35], v[132:135]\n\t"
    "v_mfma_f32_16x16x32_bf16 v[132:135], a[140:143], v[36:39], v[132:135]\n\t"
    "v_mfma_f32_16x16x32_bf16 v[160:163], a[0:3], v[60:63], 0\n\t"
    "v_mfma_f32_16x16x32_bf16 v[160:163], a[12:15], v[32:35], v[160:163]\n\t"
    "v_mfma_f32_16x16x32_bf16 v[160:163], a[24:27], v[36:39], v[160:163]\n\t"
    "v_mfma_f32_16x16x32_bf16 v[164:167], a[4:7], v[60:63], 0\n\t"
    "v_mfma_f32_16x16x32_bf16 v[164:167], a[16:19], v[32:35], v[164:167]\n\t"
    "v_mfma_f32_16x16x32_bf16 v[164:167], a[28:31], v[36:39], v[164:167]\n\t"
    "v_mfma_f32_16x16x32_bf16 v[168:171], a[8:11], v[60:63], 0\n\t"
    "v_mfma_f32_16x16x32_bf16 v[168:171], a[20:23], v[32:35], v[168:171]\n\t"
    "v_mfma_f32_16x16x32_bf16 v[168:171], a[32:35], v[36:39], v[168:171]\n\t"
    "s_waitcnt lgkmcnt(2)\n\t"
    "v_mfma_f32_16x16x32_bf16 v[88:91], a[252:255], v[40:43], v[88:91]\n\t"
    "s_waitcnt lgkmcnt(1)\n\t"
    "v_mfma_f32_16x16x32_bf16 v[88:91], v[196:199], v[44:47], v[88:91]\n\t"
    "s_waitcnt lgkmcnt(0)\n\t"
    "v_mfma_f32_16x16x32_bf16 v[88:91], v[208:211], v[48:51], v[88:91]\n\t"
    "v_mfma_f32_16x16x32_bf16 v[92:95], v[188:191], v[40:43], v[92:95]\n\t"
    "v_mfma_f32_16x16x32_bf16 v[92:95], v[200:203], v[44:47], v[92:95]\n\t"
    "v_mfma_f32_16x16x32_bf16 v[92:95], v[212:215], v[48:51], v[92:95]\n\t"
    "v_mfma_f32_16x16x32_bf16 v[96:99], v[192:195], v[40:43], v[96:99]\n\t"
    "v_mfma_f32_16x16x32_bf16 v[96:99], v[204:207], v[44:47], v[96:99]\n\t"
    "v_mfma_f32_16x16x32_bf16 v[96:99], v[216:219], v[48:51], v[96:99]\n\t"
    "v_mfma_f32_16x16x32_bf16 v[124:127], a[144:147], v[40:43], v[124:127]\n\t"
    "v_mfma_f32_16x16x32_bf16 v[124:127], a[156:159], v[44:47], v[124:127]\n\t"
    "v_mfma_f32_16x16x32_bf16 v[124:127], a[168:171], v[48:51], v[124:127]\n\t"
    "v_mfma_f32_16x16x32_bf16 v[128:131], a[148:151], v[40:43], v[128:131]\n\t"
    "ds_read_b128 v[52:55], v9 offset:26624\n\t"
    "v_mfma_f32_16x16x32_bf16 v[128:131], a[160:163], v[44:47], v[128:131]\n\t"
    "ds_read_b128 v[56:59], v9 offset:26688\n\t"
    "v_mfma_f32_16x16x32_bf16 v[128:131], a[172:175], v[48:51], v[128:131]\n\t"
    "ds_read_b128 v[60:63], v9 offset:26752\n\t"
    "v_mfma_f32_16x16x32_bf16 v[132:135], a[152:155], v[40:43], v[132:135]\n\t"
    "v_mfma_f32_16x16x32_bf16 v[132:135], a[164:167], v[44:47], v[132:135]\n\t"
    "v_mfma_f32_16x16x32_bf16 v[132:135], a[176:179], v[48:51], v[132:135]\n\t"
    "v_mfma_f32_16x16x32_bf16 v[160:163], a[36:39], v[40:43], v[160:163]\n\t"
    "v_mfma_f32_16x16x32_bf16 v[160:163], a[48:51], v[44:47], v[160:163]\n\t"
    "v_mfma_f32_16x16x32_bf16 v[160:163], a[60:63], v[48:51], v[160:163]\n\t"
    "v_mfma_f32_16x16x32_bf16 v[164:167], a[40:43], v[40:43], v[164:167]\n\t"
    "v_mfma_f32_16x16x32_bf16 v[164:167], a[52:55], v[44:47], v[164:167]\n\t"
    "v_mfma_f32_16x16x32_bf16 v[164:167], a[64:67], v[48:51], v[164:167]\n\t"
    "v_mfma_f32_16x16x32_bf16 v[168:171], a[44:47], v[40:43], v[168:171]\n\t"
    "v_mfma_f32_16x16x32_bf16 v[168:171], a[56:59], v[44:47], v[168:171]\n\t"
    "v_mfma_f32_16x16x32_bf16 v[168:171], a[68:71], v[48:51], v[168:171]\n\t"
    "s_waitcnt lgkmcnt(2)\n\t"
    "v_mfma_f32_16x16x32_bf16 v[88:91], v[220:223], v[52:55], v[88:91]\n\t"
    "s_waitcnt lgkmcnt(1)\n\t"
    "v_mfma_f32_16x16x32_bf16 v[88:91], v[232:235], v[56:59], v[88:91]\n\t"
    "s_waitcnt lgkmcnt(0)\n\t"
    "v_mfma_f32_16x16x32_bf16 v[88:91], v[244:247], v[60:63], v[88:91]\n\t"
    "v_mfma_f32_16x16x32_bf16 v[92:95], v[224:227], v[52:55], v[92:95]\n\t"
    "v_mfma_f32_16x16x32_bf16 v[92:95], v[236:239], v[56:59], v[92:95]\n\t"
    "v_mfma_f32_16x16x32_bf16 v[92:95], v[248:251], v[60:63], v[92:95]\n\t"
    "v_mfma_f32_16x16x32_bf16 v[96:99], v[228:231], v[52:55], v[96:99]\n\t"
    "v_mfma_f32_16x16x32_bf16 v[96:99], v[240:243], v[56:59], v[96:99]\n\t"
    "v_mfma_f32_16x16x32_bf16 v[96:99], v[252:255], v[60:63], v[96:99]\n\t"
    "v_mfma_f32_16x16x32_bf16 v[124:127], a[180:183], v[52:55], v[124:127]\n\t"
    "v_mfma_f32_16x16x32_bf16 v[124:127], a[192:195], v[56:59], v[124:127]\n\t"
    "s_waitcnt vmcnt(8)\n\t"
    "s_barrier\n\t"
    "v_mfma_f32_16x16x32_bf16 v[124:127], a[204:207], v[60:63], v[124:127]\n\t"
    "v_mfma_f32_16x16x32_bf16 v[128:131], a[184:187], v[52:55], v[128:131]\n\t"
    "ds_read_b128 v[32:35], v7 offset:40960\n\t"
    "v_mfma_f32_16x16x32_bf16 v[128:131], a[196:199], v[56:59], v[128:131]\n\t"
    "ds_read_b128 v[36:39], v7 offset:41024\n\t"
    "v_mfma_f32_16x16x32_bf16 v[128:131], a[208:211], v[60:63], v[128:131]\n\t"
    "ds_read_b128 v[40:43], v7 offset:41088\n\t"
    "v_mfma_f32_16x16x32_bf16 v[132:135], a[188:191], v[52:55], v[132:135]\n\t"
    "v_mfma_f32_16x16x32_bf16 v[132:135], a[200:203], v[56:59], v[132:135]\n\t"
    "s_add_u32 s78, s47, 6144\n\t"
    "v_mfma_f32_16x16x32_bf16 v[132:135], a[212:215], v[60:63], v[132:135]\n\t"
    "v_cvt_pk_bf16_f32 v182, v96, v97\n\t"
    "v_mfma_f32_16x16x32_bf16 v[160:163], a[72:75], v[52:55], v[160:163]\n\t"
    "v_cvt_pk_bf16_f32 v183, v98, v99\n\t"
    "v_mfma_f32_16x16x32_bf16 v[160:163], a[84:87], v[56:59], v[160:163]\n\t"
    "ds_write_b64 v25, v[182:183] offset:3840\n\t"
    "v_mfma_f32_16x16x32_bf16 v[160:163], a[96:99], v[60:63], v[160:163]\n\t"
    "v_cvt_pk_bf16_f32 v178, v88, v89\n\t"
    "v_mfma_f32_16x16x32_bf16 v[164:167], a[76:79], v[52:55], v[164:167]\n\t"
    "v_cvt_pk_bf16_f32 v179, v90, v91\n\t"
    "v_mfma_f32_16x16x32_bf16 v[164:167], a[88:91], v[56:59], v[164:167]\n\t"
    "v_cvt_pk_bf16_f32 v180, v92, v93\n\t"
    "v_mfma_f32_16x16x32_bf16 v[164:167], a[100:103], v[60:63], v[164:167]\n\t"
    "v_cvt_pk_bf16_f32 v181, v94, v95\n\t"
    "v_mfma_f32_16x16x32_bf16 v[168:171], a[80:83], v[52:55], v[168:171]\n\t"
    "buffer_store_dwordx4 v[178:181], v22, s[28:31], s78 offen nt\n\t"
    "v_mfma_f32_16x16x32_bf16 v[168:171], a[92:95], v[56:59], v[168:171]\n\t"
    "v_mfma_f32_16x16x32_bf16 v[168:171], a[104:107], v[60:63], v[168:171]\n\t"
    "s_waitcnt lgkmcnt(3)\n\t"
    "v_mfma_f32_16x16x32_bf16 v[100:103], a[216:219], v[32:35], v[100:103]\n\t"
    "s_waitcnt lgkmcnt(2)\n\t"
    "v_mfma_f32_16x16x32_bf16 v[100:103], a[228:231], v[36:39], v[100:103]\n\t"
    "s_waitcnt lgkmcnt(0)\n\t"
    "s_barrier\n\t"
    "v_mfma_f32_16x16x32_bf16 v[100:103], a[240:243], v[40:43], v[100:103]\n\t"
    "ds_read_b128 v[28:31], v26 offset:2560\n\t"
    "v_mfma_f32_16x16x32_bf16 v[104:107], a[220:223], v[32:35], v[104:107]\n\t"
    "s_add_u32 s96, s47, 3072\n\t"
    "s_add_u32 s97, s47, 6144\n\t"
    "ds_read_b128 v[44:47], v8 offset:40960\n\t"
    "v_mfma_f32_16x16x32_bf16 v[104:107], a[232:235], v[36:39], v[104:107]\n\t"
    "s_cmp_eq_u32 s98, 0\n\t"
    "s_cselect_b32 s96, s96, s97\n\t"
    "ds_read_b128 v[48:51], v8 offset:41024\n\t"
    "v_mfma_f32_16x16x32_bf16 v[104:107], a[244:247], v[40:43], v[104:107]\n\t"
    "ds_read_b128 v[52:55], v8 offset:41088\n\t"
    "v_mfma_f32_16x16x32_bf16 v[108:111], a[224:227], v[32:35], v[108:111]\n\t"
    "v_mfma_f32_16x16x32_bf16 v[108:111], a[236:239], v[36:39], v[108:111]\n\t"
    "v_mfma_f32_16x16x32_bf16 v[108:111], a[248:251], v[40:43], v[108:111]\n\t"
    "v_mfma_f32_16x16x32_bf16 v[136:139], a[108:111], v[32:35], v[136:139]\n\t"
    "v_mfma_f32_16x16x32_bf16 v[136:139], a[120:123], v[36:39], v[136:139]\n\t"
    "v_mfma_f32_16x16x32_bf16 v[136:139], a[132:135], v[40:43], v[136:139]\n\t"
    "v_mfma_f32_16x16x32_bf16 v[140:143], a[112:115], v[32:35], v[140:143]\n\t"
    "v_mfma_f32_16x16x32_bf16 v[140:143], a[124:127], v[36:39], v[140:143]\n\t"
    "v_mfma_f32_16x16x32_bf16 v[140:143], a[136:139], v[40:43], v[140:143]\n\t"
    "s_waitcnt lgkmcnt(3)\n\t"
    "buffer_store_dwordx4 v[28:31], v23, s[28:31], s96 offen nt\n\t"
    "v_mfma_f32_16x16x32_bf16 v[144:147], a[116:119], v[32:35], v[144:147]\n\t"
    "v_mfma_f32_16x16x32_bf16 v[144:147], a[128:131], v[36:39], v[144:147]\n\t"
    "v_mfma_f32_16x16x32_bf16 v[144:147], a[140:143], v[40:43], v[144:147]\n\t"
    "s_waitcnt lgkmcnt(2)\n\t"
    "v_mfma_f32_16x16x32_bf16 v[100:103], a[252:255], v[44:47], v[100:103]\n\t"
    "s_waitcnt lgkmcnt(1)\n\t"
    "v_mfma_f32_16x16x32_bf16 v[100:103], v[196:199], v[48:51], v[100:103]\n\t"
    "s_waitcnt lgkmcnt(0)\n\t"
    "v_mfma_f32_16x16x32_bf16 v[100:103], v[208:211], v[52:55], v[100:103]\n\t"
    "v_mfma_f32_16x16x32_bf16 v[104:107], v[188:191], v[44:47], v[104:107]\n\t"
    "ds_read_b128 v[56:59], v9 offset:40960\n\t"
    "v_mfma_f32_16x16x32_bf16 v[104:107], v[200:203], v[48:51], v[104:107]\n\t"
    "ds_read_b128 v[60:63], v9 offset:41024\n\t"
    "v_mfma_f32_16x16x32_bf16 v[104:107], v[212:215], v[52:55], v[104:107]\n\t"
    "ds_read_b128 v[32:35], v9 offset:41088\n\t"
    "v_mfma_f32_16x16x32_bf16 v[108:111], v[192:195], v[44:47], v[108:111]\n\t"
    "v_mfma_f32_16x16x32_bf16 v[108:111], v[204:207], v[48:51], v[108:111]\n\t"
    "v_mfma_f32_16x16x32_bf16 v[108:111], v[216:219], v[52:55], v[108:111]\n\t"
    "v_mfma_f32_16x16x32_bf16 v[136:139], a[144:147], v[44:47], v[136:139]\n\t"
    "v_mfma_f32_16x16x32_bf16 v[136:139], a[156:159], v[48:51], v[136:139]\n\t"
    "v_mfma_f32_16x16x32_bf16 v[136:139], a[168:171], v[52:55], v[136:139]\n\t"
    "v_mfma_f32_16x16x32_bf16 v[140:143], a[148:151], v[44:47], v[140:143]\n\t"
    "v_mfma_f32_16x16x32_bf16 v[140:143], a[160:163], v[48:51], v[140:143]\n\t"
    "v_mfma_f32_16x16x32_bf16 v[140:143], a[172:175], v[52:55], v[140:143]\n\t"
    "v_mfma_f32_16x16x32_bf16 v[144:147], a[152:155], v[44:47], v[144:147]\n\t"
    "v_mfma_f32_16x16x32_bf16 v[144:147], a[164:167], v[48:51], v[144:147]\n\t"
    "v_mfma_f32_16x16x32_bf16 v[144:147], a[176:179], v[52:55], v[144:147]\n\t"
    "s_waitcnt lgkmcnt(2)\n\t"
    "v_mfma_f32_16x16x32_bf16 v[100:103], v[220:223], v[56:59], v[100:103]\n\t"
    "s_waitcnt lgkmcnt(1)\n\t"
    "v_mfma_f32_16x16x32_bf16 v[100:103], v[232:235], v[60:63], v[100:103]\n\t"
    "s_waitcnt lgkmcnt(0)\n\t"
    "v_mfma_f32_16x16x32_bf16 v[100:103], v[244:247], v[32:35], v[100:103]\n\t"
    "v_mfma_f32_16x16x32_bf16 v[104:107], v[224:227], v[56:59], v[104:107]\n\t"
    "ds_read_b128 v[36:39], v7 offset:44032\n\t"
    "v_mfma_f32_16x16x32_bf16 v[104:107], v[236:239], v[60:63], v[104:107]\n\t"
    "ds_read_b128 v[40:43], v7 offset:44096\n\t"
    "v_mfma_f32_16x16x32_bf16 v[104:107], v[248:251], v[32:35], v[104:107]\n\t"
    "ds_read_b128 v[44:47], v7 offset:44160\n\t"
    "v_mfma_f32_16x16x32_bf16 v[108:111], v[228:231], v[56:59], v[108:111]\n\t"
    "v_mfma_f32_16x16x32_bf16 v[108:111], v[240:243], v[60:63], v[108:111]\n\t"
    "v_mfma_f32_16x16x32_bf16 v[108:111], v[252:255], v[32:35], v[108:111]\n\t"
    "v_mfma_f32_16x16x32_bf16 v[136:139], a[180:183], v[56:59], v[136:139]\n\t"
    "v_mfma_f32_16x16x32_bf16 v[136:139], a[192:195], v[60:63], v[136:139]\n\t"
    "v_mfma_f32_16x16x32_bf16 v[136:139], a[204:207], v[32:35], v[136:139]\n\t"
    "v_mfma_f32_16x16x32_bf16 v[140:143], a[184:187], v[56:59], v[140:143]\n\t"
    "v_mfma_f32_16x16x32_bf16 v[140:143], a[196:199], v[60:63], v[140:143]\n\t"
    "v_mfma_f32_16x16x32_bf16 v[140:143], a[208:211], v[32:35], v[140:143]\n\t"
    "v_mfma_f32_16x16x32_bf16 v[144:147], a[188:191], v[56:59], v[144:147]\n\t"
    "v_mfma_f32_16x16x32_bf16 v[144:147], a[200:203], v[60:63], v[144:147]\n\t"
    "s_add_u32 s77, s48, 0\n\t"
    "v_mfma_f32_16x16x32_bf16 v[144:147], a[212:215], v[32:35], v[144:147]\n\t"
    "v_cvt_pk_bf16_f32 v176, v108, v109\n\t"
    "s_waitcnt lgkmcnt(2)\n\t"
    "v_mfma_f32_16x16x32_bf16 v[112:115], a[216:219], v[36:39], v[112:115]\n\t"
    "v_cvt_pk_bf16_f32 v177, v110, v111\n\t"
    "s_waitcnt lgkmcnt(1)\n\t"
    "v_mfma_f32_16x16x32_bf16 v[112:115], a[228:231], v[40:43], v[112:115]\n\t"
    "ds_write_b64 v25, v[176:177] offset:0\n\t"
    "s_waitcnt lgkmcnt(1)\n\t"
    "v_mfma_f32_16x16x32_bf16 v[112:115], a[240:243], v[44:47], v[112:115]\n\t"
    "v_cvt_pk_bf16_f32 v172, v100, v101\n\t"
    "v_mfma_f32_16x16x32_bf16 v[116:119], a[220:223], v[36:39], v[116:119]\n\t"
    "v_cvt_pk_bf16_f32 v173, v102, v103\n\t"
    "ds_read_b128 v[48:51], v8 offset:44032\n\t"
    "v_mfma_f32_16x16x32_bf16 v[116:119], a[232:235], v[40:43], v[116:119]\n\t"
    "v_cvt_pk_bf16_f32 v174, v104, v105\n\t"
    "ds_read_b128 v[52:55], v8 offset:44096\n\t"
    "v_mfma_f32_16x16x32_bf16 v[116:119], a[244:247], v[44:47], v[116:119]\n\t"
    "v_cvt_pk_bf16_f32 v175, v106, v107\n\t"
    "ds_read_b128 v[56:59], v8 offset:44160\n\t"
    "v_mfma_f32_16x16x32_bf16 v[120:123], a[224:227], v[36:39], v[120:123]\n\t"
    "buffer_store_dwordx4 v[172:175], v22, s[28:31], s77 offen nt\n\t"
    "v_mfma_f32_16x16x32_bf16 v[120:123], a[236:239], v[40:43], v[120:123]\n\t"
    "v_mfma_f32_16x16x32_bf16 v[120:123], a[248:251], v[44:47], v[120:123]\n\t"
    "v_mfma_f32_16x16x32_bf16 v[148:151], a[108:111], v[36:39], v[148:151]\n\t"
    "v_mfma_f32_16x16x32_bf16 v[148:151], a[120:123], v[40:43], v[148:151]\n\t"
    "v_mfma_f32_16x16x32_bf16 v[148:151], a[132:135], v[44:47], v[148:151]\n\t"
    "v_mfma_f32_16x16x32_bf16 v[152:155], a[112:115], v[36:39], v[152:155]\n\t"
    "v_mfma_f32_16x16x32_bf16 v[152:155], a[124:127], v[40:43], v[152:155]\n\t"
    "v_mfma_f32_16x16x32_bf16 v[152:155], a[136:139], v[44:47], v[152:155]\n\t"
    "v_mfma_f32_16x16x32_bf16 v[156:159], a[116:119], v[36:39], v[156:159]\n\t"
    "v_mfma_f32_16x16x32_bf16 v[156:159], a[128:131], v[40:43], v[156:159]\n\t"
    "v_mfma_f32_16x16x32_bf16 v[156:159], a[140:143], v[44:47], v[156:159]\n\t"
    "s_waitcnt lgkmcnt(2)\n\t"
    "v_mfma_f32_16x16x32_bf16 v[112:115], a[252:255], v[48:51], v[112:115]\n\t"
    "s_waitcnt lgkmcnt(1)\n\t"
    "v_mfma_f32_16x16x32_bf16 v[112:115], v[196:199], v[52:55], v[112:115]\n\t"
    "s_waitcnt lgkmcnt(0)\n\t"
    "v_mfma_f32_16x16x32_bf16 v[112:115], v[208:211], v[56:59], v[112:115]\n\t"
    "v_mfma_f32_16x16x32_bf16 v[116:119], v[188:191], v[48:51], v[116:119]\n\t"
    "ds_read_b128 v[60:63], v9 offset:44032\n\t"
    "v_mfma_f32_16x16x32_bf16 v[116:119], v[200:203], v[52:55], v[116:119]\n\t"
    "ds_read_b128 v[32:35], v9 offset:44096\n\t"
    "v_mfma_f32_16x16x32_bf16 v[116:119], v[212:215], v[56:59], v[116:119]\n\t"
    "ds_read_b128 v[36:39], v9 offset:44160\n\t"
    "v_mfma_f32_16x16x32_bf16 v[120:123], v[192:195], v[48:51], v[120:123]\n\t"
    "v_mfma_f32_16x16x32_bf16 v[120:123], v[204:207], v[52:55], v[120:123]\n\t"
    "v_mfma_f32_16x16x32_bf16 v[120:123], v[216:219], v[56:59], v[120:123]\n\t"
    "v_mfma_f32_16x16x32_bf16 v[148:151], a[144:147], v[48:51], v[148:151]\n\t"
    "v_mfma_f32_16x16x32_bf16 v[148:151], a[156:159], v[52:55], v[148:151]\n\t"
    "v_mfma_f32_16x16x32_bf16 v[148:151], a[168:171], v[56:59], v[148:151]\n\t"
    "v_mfma_f32_16x16x32_bf16 v[152:155], a[148:151], v[48:51], v[152:155]\n\t"
    "v_mfma_f32_16x16x32_bf16 v[152:155], a[160:163], v[52:55], v[152:155]\n\t"
    "v_mfma_f32_16x16x32_bf16 v[152:155], a[172:175], v[56:59], v[152:155]\n\t"
    "v_mfma_f32_16x16x32_bf16 v[156:159], a[152:155], v[48:51], v[156:159]\n\t"
    "v_mfma_f32_16x16x32_bf16 v[156:159], a[164:167], v[52:55], v[156:159]\n\t"
    "v_mfma_f32_16x16x32_bf16 v[156:159], a[176:179], v[56:59], v[156:159]\n\t"
    "s_waitcnt lgkmcnt(2)\n\t"
    "v_mfma_f32_16x16x32_bf16 v[112:115], v[220:223], v[60:63], v[112:115]\n\t"
    "s_waitcnt lgkmcnt(1)\n\t"
    "v_mfma_f32_16x16x32_bf16 v[112:115], v[232:235], v[32:35], v[112:115]\n\t"
    "s_waitcnt lgkmcnt(0)\n\t"
    "v_mfma_f32_16x16x32_bf16 v[112:115], v[244:247], v[36:39], v[112:115]\n\t"
    "v_mfma_f32_16x16x32_bf16 v[116:119], v[224:227], v[60:63], v[116:119]\n\t"
    "ds_read_b128 v[40:43], v7 offset:47104\n\t"
    "v_mfma_f32_16x16x32_bf16 v[116:119], v[236:239], v[32:35], v[116:119]\n\t"
    "ds_read_b128 v[44:47], v7 offset:47168\n\t"
    "v_mfma_f32_16x16x32_bf16 v[116:119], v[248:251], v[36:39], v[116:119]\n\t"
    "ds_read_b128 v[48:51], v7 offset:47232\n\t"
    "v_mfma_f32_16x16x32_bf16 v[120:123], v[228:231], v[60:63], v[120:123]\n\t"
    "v_mfma_f32_16x16x32_bf16 v[120:123], v[240:243], v[32:35], v[120:123]\n\t"
    "v_mfma_f32_16x16x32_bf16 v[120:123], v[252:255], v[36:39], v[120:123]\n\t"
    "v_mfma_f32_16x16x32_bf16 v[148:151], a[180:183], v[60:63], v[148:151]\n\t"
    "v_mfma_f32_16x16x32_bf16 v[148:151], a[192:195], v[32:35], v[148:151]\n\t"
    "v_mfma_f32_16x16x32_bf16 v[148:151], a[204:207], v[36:39], v[148:151]\n\t"
    "v_mfma_f32_16x16x32_bf16 v[152:155], a[184:187], v[60:63], v[152:155]\n\t"
    "v_mfma_f32_16x16x32_bf16 v[152:155], a[196:199], v[32:35], v[152:155]\n\t"
    "v_mfma_f32_16x16x32_bf16 v[152:155], a[208:211], v[36:39], v[152:155]\n\t"
    "v_mfma_f32_16x16x32_bf16 v[156:159], a[188:191], v[60:63], v[156:159]\n\t"
    "v_mfma_f32_16x16x32_bf16 v[156:159], a[200:203], v[32:35], v[156:159]\n\t"
    "s_add_u32 s78, s48, 3072\n\t"
    "v_mfma_f32_16x16x32_bf16 v[156:159], a[212:215], v[36:39], v[156:159]\n\t"
    "v_cvt_pk_bf16_f32 v182, v120, v121\n\t"
    "s_waitcnt lgkmcnt(2)\n\t"
    "v_mfma_f32_16x16x32_bf16 v[124:127], a[216:219], v[40:43], v[124:127]\n\t"
    "v_cvt_pk_bf16_f32 v183, v122, v123\n\t"
    "s_waitcnt lgkmcnt(1)\n\t"
    "v_mfma_f32_16x16x32_bf16 v[124:127], a[228:231], v[44:47], v[124:127]\n\t"
    "ds_write_b64 v25, v[182:183] offset:1280\n\t"
    "s_waitcnt lgkmcnt(1)\n\t"
    "v_mfma_f32_16x16x32_bf16 v[124:127], a[240:243], v[48:51], v[124:127]\n\t"
    "v_cvt_pk_bf16_f32 v178, v112, v113\n\t"
    "v_mfma_f32_16x16x32_bf16 v[128:131], a[220:223], v[40:43], v[128:131]\n\t"
    "v_cvt_pk_bf16_f32 v179, v114, v115\n\t"
    "ds_read_b128 v[52:55], v8 offset:47104\n\t"
    "v_mfma_f32_16x16x32_bf16 v[128:131], a[232:235], v[44:47], v[128:131]\n\t"
    "v_cvt_pk_bf16_f32 v180, v116, v117\n\t"
    "ds_read_b128 v[56:59], v8 offset:47168\n\t"
    "v_mfma_f32_16x16x32_bf16 v[128:131], a[244:247], v[48:51], v[128:131]\n\t"
    "v_cvt_pk_bf16_f32 v181, v118, v119\n\t"
    "ds_read_b128 v[60:63], v8 offset:47232\n\t"
    "v_mfma_f32_16x16x32_bf16 v[132:135], a[224:227], v[40:43], v[132:135]\n\t"
    "buffer_store_dwordx4 v[178:181], v22, s[28:31], s78 offen nt\n\t"
    "v_mfma_f32_16x16x32_bf16 v[132:135], a[236:239], v[44:47], v[132:135]\n\t"
    "v_mfma_f32_16x16x32_bf16 v[132:135], a[248:251], v[48:51], v[132:135]\n\t"
    "v_mfma_f32_16x16x32_bf16 v[160:163], a[108:111], v[40:43], v[160:163]\n\t"
    "v_mfma_f32_16x16x32_bf16 v[160:163], a[120:123], v[44:47], v[160:163]\n\t"
    "s_waitcnt lgkmcnt(3)\n\t"
    "s_barrier\n\t"
    "v_mfma_f32_16x16x32_bf16 v[160:163], a[132:135], v[48:51], v[160:163]\n\t"
    "ds_read_b128 v[28:31], v26 offset:0\n\t"
    "v_mfma_f32_16x16x32_bf16 v[164:167], a[112:115], v[40:43], v[164:167]\n\t"
    "s_add_u32 s96, s48, 0\n\t"
    "s_add_u32 s97, s48, 3072\n\t"
    "v_mfma_f32_16x16x32_bf16 v[164:167], a[124:127], v[44:47], v[164:167]\n\t"
    "s_cmp_eq_u32 s98, 0\n\t"
    "s_cselect_b32 s96, s96, s97\n\t"
    "v_mfma_f32_16x16x32_bf16 v[164:167], a[136:139], v[48:51], v[164:167]\n\t"
    "v_mfma_f32_16x16x32_bf16 v[168:171], a[116:119], v[40:43], v[168:171]\n\t"
    "v_mfma_f32_16x16x32_bf16 v[168:171], a[128:131], v[44:47], v[168:171]\n\t"
    "v_mfma_f32_16x16x32_bf16 v[168:171], a[140:143], v[48:51], v[168:171]\n\t"
    "s_waitcnt lgkmcnt(3)\n\t"
    "v_mfma_f32_16x16x32_bf16 v[124:127], a[252:255], v[52:55], v[124:127]\n\t"
    "s_waitcnt lgkmcnt(2)\n\t"
    "v_mfma_f32_16x16x32_bf16 v[124:127], v[196:199], v[56:59], v[124:127]\n\t"
    "s_waitcnt lgkmcnt(1)\n\t"
    "v_mfma_f32_16x16x32_bf16 v[124:127], v[208:211], v[60:63], v[124:127]\n\t"
    "v_mfma_f32_16x16x32_bf16 v[128:131], v[188:191], v[52:55], v[128:131]\n\t"
    "ds_read_b128 v[32:35], v9 offset:47104\n\t"
    "v_mfma_f32_16x16x32_bf16 v[128:131], v[200:203], v[56:59], v[128:131]\n\t"
    "ds_read_b128 v[36:39], v9 offset:47168\n\t"
    "v_mfma_f32_16x16x32_bf16 v[128:131], v[212:215], v[60:63], v[128:131]\n\t"
    "s_waitcnt lgkmcnt(2)\n\t"
    "buffer_store_dwordx4 v[28:31], v23, s[28:31], s96 offen nt\n\t"
    "ds_read_b128 v[40:43], v9 offset:47232\n\t"
    "v_mfma_f32_16x16x32_bf16 v[132:135], v[192:195], v[52:55], v[132:135]\n\t"
    "v_mfma_f32_16x16x32_bf16 v[132:135], v[204:207], v[56:59], v[132:135]\n\t"
    "v_mfma_f32_16x16x32_bf16 v[132:135], v[216:219], v[60:63], v[132:135]\n\t"
    "v_mfma_f32_16x16x32_bf16 v[160:163], a[144:147], v[52:55], v[160:163]\n\t"
    "v_mfma_f32_16x16x32_bf16 v[160:163], a[156:159], v[56:59], v[160:163]\n\t"
    "v_mfma_f32_16x16x32_bf16 v[160:163], a[168:171], v[60:63], v[160:163]\n\t"
    "v_mfma_f32_16x16x32_bf16 v[164:167], a[148:151], v[52:55], v[164:167]\n\t"
    "v_mfma_f32_16x16x32_bf16 v[164:167], a[160:163], v[56:59], v[164:167]\n\t"
    "v_mfma_f32_16x16x32_bf16 v[164:167], a[172:175], v[60:63], v[164:167]\n\t"
    "v_mfma_f32_16x16x32_bf16 v[168:171], a[152:155], v[52:55], v[168:171]\n\t"
    "v_mfma_f32_16x16x32_bf16 v[168:171], a[164:167], v[56:59], v[168:171]\n\t"
    "v_mfma_f32_16x16x32_bf16 v[168:171], a[176:179], v[60:63], v[168:171]\n\t"
    "s_waitcnt lgkmcnt(2)\n\t"
    "v_mfma_f32_16x16x32_bf16 v[124:127], v[220:223], v[32:35], v[124:127]\n\t"
    "s_waitcnt lgkmcnt(1)\n\t"
    "v_mfma_f32_16x16x32_bf16 v[124:127], v[232:235], v[36:39], v[124:127]\n\t"
    "s_waitcnt vmcnt(5)\n\t"
    "s_waitcnt lgkmcnt(0)\n\t"
    "s_barrier\n\t"
    "v_mfma_f32_16x16x32_bf16 v[124:127], v[244:247], v[40:43], v[124:127]\n\t"
    "v_mfma_f32_16x16x32_bf16 v[128:131], v[224:227], v[32:35], v[128:131]\n\t"
    "ds_read_b128 v[44:47], v10 offset:0\n\t"
    "v_mfma_f32_16x16x32_bf16 v[128:131], v[236:239], v[36:39], v[128:131]\n\t"
    "ds_read_b128 v[48:51], v10 offset:64\n\t"
    "v_mfma_f32_16x16x32_bf16 v[128:131], v[248:251], v[40:43], v[128:131]\n\t"
    "ds_read_b128 v[52:55], v10 offset:128\n\t"
    "v_mfma_f32_16x16x32_bf16 v[132:135], v[228:231], v[32:35], v[132:135]\n\t"
    "v_mfma_f32_16x16x32_bf16 v[132:135], v[240:243], v[36:39], v[132:135]\n\t"
    "v_mfma_f32_16x16x32_bf16 v[132:135], v[252:255], v[40:43], v[132:135]\n\t"
    "v_mfma_f32_16x16x32_bf16 v[160:163], a[180:183], v[32:35], v[160:163]\n\t"
    "v_mfma_f32_16x16x32_bf16 v[160:163], a[192:195], v[36:39], v[160:163]\n\t"
    "v_mfma_f32_16x16x32_bf16 v[160:163], a[204:207], v[40:43], v[160:163]\n\t"
    "v_mfma_f32_16x16x32_bf16 v[164:167], a[184:187], v[32:35], v[164:167]\n\t"
    "ds_read_b128 v[56:59], v11 offset:0\n\t"
    "v_mfma_f32_16x16x32_bf16 v[164:167], a[196:199], v[36:39], v[164:167]\n\t"
    "ds_read_b128 v[60:63], v11 offset:64\n\t"
    "v_mfma_f32_16x16x32_bf16 v[164:167], a[208:211], v[40:43], v[164:167]\n\t"
    "v_mfma_f32_16x16x32_bf16 v[168:171], a[188:191], v[32:35], v[168:171]\n\t"
    "ds_read_b128 v[32:35], v11 offset:128\n\t"
    "v_mfma_f32_16x16x32_bf16 v[168:171], a[200:203], v[36:39], v[168:171]\n\t"
    "s_add_u32 s77, s48, 6144\n\t"
    "v_mfma_f32_16x16x32_bf16 v[168:171], a[212:215], v[40:43], v[168:171]\n\t"
    "v_cvt_pk_bf16_f32 v176, v132, v133\n\t"
    "s_waitcnt lgkmcnt(5)\n\t"
    "v_mfma_f32_16x16x32_bf16 v[136:139], a[216:219], v[44:47], v[136:139]\n\t"
    "v_cvt_pk_bf16_f32 v177, v134, v135\n\t"
    "s_waitcnt lgkmcnt(4)\n\t"
    "v_mfma_f32_16x16x32_bf16 v[136:139], a[228:231], v[48:51], v[136:139]\n\t"
    "ds_write_b64 v25, v[176:177] offset:2560\n\t"
    "s_waitcnt lgkmcnt(4)\n\t"
    "v_mfma_f32_16x16x32_bf16 v[136:139], a[240:243], v[52:55], v[136:139]\n\t"
    "v_cvt_pk_bf16_f32 v172, v124, v125\n\t"
    "v_mfma_f32_16x16x32_bf16 v[140:143], a[220:223], v[44:47], v[140:143]\n\t"
    "v_cvt_pk_bf16_f32 v173, v126, v127\n\t"
    "ds_read_b128 v[36:39], v12 offset:0\n\t"
    "v_mfma_f32_16x16x32_bf16 v[140:143], a[232:235], v[48:51], v[140:143]\n\t"
    "v_cvt_pk_bf16_f32 v174, v128, v129\n\t"
    "ds_read_b128 v[40:43], v12 offset:64\n\t"
    "v_mfma_f32_16x16x32_bf16 v[140:143], a[244:247], v[52:55], v[140:143]\n\t"
    "v_cvt_pk_bf16_f32 v175, v130, v131\n\t"
    "v_mfma_f32_16x16x32_bf16 v[144:147], a[224:227], v[44:47], v[144:147]\n\t"
    "buffer_store_dwordx4 v[172:175], v22, s[28:31], s77 offen nt\n\t"
    "ds_read_b128 v[44:47], v12 offset:128\n\t"
    "v_mfma_f32_16x16x32_bf16 v[144:147], a[236:239], v[48:51], v[144:147]\n\t"
    "v_mfma_f32_16x16x32_bf16 v[144:147], a[248:251], v[52:55], v[144:147]\n\t"
    "s_waitcnt lgkmcnt(6)\n\t"
    "v_mfma_f32_16x16x32_bf16 v[136:139], a[252:255], v[56:59], v[136:139]\n\t"
    "s_waitcnt lgkmcnt(5)\n\t"
    "v_mfma_f32_16x16x32_bf16 v[136:139], v[196:199], v[60:63], v[136:139]\n\t"
    "s_waitcnt lgkmcnt(4)\n\t"
    "v_mfma_f32_16x16x32_bf16 v[136:139], v[208:211], v[32:35], v[136:139]\n\t"
    "v_mfma_f32_16x16x32_bf16 v[140:143], v[188:191], v[56:59], v[140:143]\n\t"
    "ds_read_b128 v[48:51], v10 offset:3072\n\t"
    "v_mfma_f32_16x16x32_bf16 v[140:143], v[200:203], v[60:63], v[140:143]\n\t"
    "ds_read_b128 v[52:55], v10 offset:3136\n\t"
    "v_mfma_f32_16x16x32_bf16 v[140:143], v[212:215], v[32:35], v[140:143]\n\t"
    "v_mfma_f32_16x16x32_bf16 v[144:147], v[192:195], v[56:59], v[144:147]\n\t"
    "ds_read_b128 v[56:59], v10 offset:3200\n\t"
    "v_mfma_f32_16x16x32_bf16 v[144:147], v[204:207], v[60:63], v[144:147]\n\t"
    "v_mfma_f32_16x16x32_bf16 v[144:147], v[216:219], v[32:35], v[144:147]\n\t"
    "s_waitcnt lgkmcnt(5)\n\t"
    "v_mfma_f32_16x16x32_bf16 v[136:139], v[220:223], v[36:39], v[136:139]\n\t"
    "s_waitcnt lgkmcnt(4)\n\t"
    "v_mfma_f32_16x16x32_bf16 v[136:139], v[232:235], v[40:43], v[136:139]\n\t"
    "s_waitcnt lgkmcnt(3)\n\t"
    "v_mfma_f32_16x16x32_bf16 v[136:139], v[244:247], v[44:47], v[136:139]\n\t"
    "v_mfma_f32_16x16x32_bf16 v[140:143], v[224:227], v[36:39], v[140:143]\n\t"
    "ds_read_b128 v[60:63], v11 offset:3072\n\t"
    "v_mfma_f32_16x16x32_bf16 v[140:143], v[236:239], v[40:43], v[140:143]\n\t"
    "ds_read_b128 v[32:35], v11 offset:3136\n\t"
    "v_mfma_f32_16x16x32_bf16 v[140:143], v[248:251], v[44:47], v[140:143]\n\t"
    "v_mfma_f32_16x16x32_bf16 v[144:147], v[228:231], v[36:39], v[144:147]\n\t"
    "ds_read_b128 v[36:39], v11 offset:3200\n\t"
    "v_mfma_f32_16x16x32_bf16 v[144:147], v[240:243], v[40:43], v[144:147]\n\t"
    "v_mfma_f32_16x16x32_bf16 v[144:147], v[252:255], v[44:47], v[144:147]\n\t"
    "s_waitcnt lgkmcnt(5)\n\t"
    "v_mfma_f32_16x16x32_bf16 v[148:151], a[216:219], v[48:51], v[148:151]\n\t"
    "s_waitcnt lgkmcnt(4)\n\t"
    "v_mfma_f32_16x16x32_bf16 v[148:151], a[228:231], v[52:55], v[148:151]\n\t"
    "s_waitcnt lgkmcnt(3)\n\t"
    "v_mfma_f32_16x16x32_bf16 v[148:151], a[240:243], v[56:59], v[148:151]\n\t"
    "v_mfma_f32_16x16x32_bf16 v[152:155], a[220:223], v[48:51], v[152:155]\n\t"
    "ds_read_b128 v[40:43], v12 offset:3072\n\t"
    "v_mfma_f32_16x16x32_bf16 v[152:155], a[232:235], v[52:55], v[152:155]\n\t"
    "ds_read_b128 v[44:47], v12 offset:3136\n\t"
    "v_mfma_f32_16x16x32_bf16 v[152:155], a[244:247], v[56:59], v[152:155]\n\t"
    "v_mfma_f32_16x16x32_bf16 v[156:159], a[224:227], v[48:51], v[156:159]\n\t"
    "ds_read_b128 v[48:51], v12 offset:3200\n\t"
    "v_mfma_f32_16x16x32_bf16 v[156:159], a[236:239], v[52:55], v[156:159]\n\t"
    "s_add_u32 s78, s49, 0\n\t"
    "v_mfma_f32_16x16x32_bf16 v[156:159], a[248:251], v[56:59], v[156:159]\n\t"
    "v_cvt_pk_bf16_f32 v182, v144, v145\n\t"
    "s_waitcnt lgkmcnt(5)\n\t"
    "v_mfma_f32_16x16x32_bf16 v[148:151], a[252:255], v[60:63], v[148:151]\n\t"
    "v_cvt_pk_bf16_f32 v183, v146, v147\n\t"
    "s_waitcnt lgkmcnt(4)\n\t"
    "v_mfma_f32_16x16x32_bf16 v[148:151], v[196:199], v[32:35], v[148:151]\n\t"
    "ds_write_b64 v25, v[182:183] offset:3840\n\t"
    "s_waitcnt lgkmcnt(4)\n\t"
    "v_mfma_f32_16x16x32_bf16 v[148:151], v[208:211], v[36:39], v[148:151]\n\t"
    "v_cvt_pk_bf16_f32 v178, v136, v137\n\t"
    "v_mfma_f32_16x16x32_bf16 v[152:155], v[188:191], v[60:63], v[152:155]\n\t"
    "v_cvt_pk_bf16_f32 v179, v138, v139\n\t"
    "ds_read_b128 v[52:55], v10 offset:6144\n\t"
    "v_mfma_f32_16x16x32_bf16 v[152:155], v[200:203], v[32:35], v[152:155]\n\t"
    "v_cvt_pk_bf16_f32 v180, v140, v141\n\t"
    "ds_read_b128 v[56:59], v10 offset:6208\n\t"
    "v_mfma_f32_16x16x32_bf16 v[152:155], v[212:215], v[36:39], v[152:155]\n\t"
    "v_cvt_pk_bf16_f32 v181, v142, v143\n\t"
    "v_mfma_f32_16x16x32_bf16 v[156:159], v[192:195], v[60:63], v[156:159]\n\t"
    "buffer_store_dwordx4 v[178:181], v22, s[28:31], s78 offen nt\n\t"
    "ds_read_b128 v[60:63], v10 offset:6272\n\t"
    "v_mfma_f32_16x16x32_bf16 v[156:159], v[204:207], v[32:35], v[156:159]\n\t"
    "v_mfma_f32_16x16x32_bf16 v[156:159], v[216:219], v[36:39], v[156:159]\n\t"
    "s_waitcnt lgkmcnt(6)\n\t"
    "v_mfma_f32_16x16x32_bf16 v[148:151], v[220:223], v[40:43], v[148:151]\n\t"
    "s_waitcnt lgkmcnt(5)\n\t"
    "v_mfma_f32_16x16x32_bf16 v[148:151], v[232:235], v[44:47], v[148:151]\n\t"
    "s_waitcnt lgkmcnt(3)\n\t"
    "s_barrier\n\t"
    "v_mfma_f32_16x16x32_bf16 v[148:151], v[244:247], v[48:51], v[148:151]\n\t"
    "ds_read_b128 v[28:31], v26 offset:2560\n\t"
    "v_mfma_f32_16x16x32_bf16 v[152:155], v[224:227], v[40:43], v[152:155]\n\t"
    "s_add_u32 s96, s48, 6144\n\t"
    "s_add_u32 s97, s49, 0\n\t"
    "ds_read_b128 v[32:35], v11 offset:6144\n\t"
    "v_mfma_f32_16x16x32_bf16 v[152:155], v[236:239], v[44:47], v[152:155]\n\t"
    "s_cmp_eq_u32 s98, 0\n\t"
    "s_cselect_b32 s96, s96, s97\n\t"
    "ds_read_b128 v[36:39], v11 offset:6208\n\t"
    "v_mfma_f32_16x16x32_bf16 v[152:155], v[248:251], v[48:51], v[152:155]\n\t"
    "v_mfma_f32_16x16x32_bf16 v[156:159], v[228:231], v[40:43], v[156:159]\n\t"
    "ds_read_b128 v[40:43], v11 offset:6272\n\t"
    "v_mfma_f32_16x16x32_bf16 v[156:159], v[240:243], v[44:47], v[156:159]\n\t"
    "v_mfma_f32_16x16x32_bf16 v[156:159], v[252:255], v[48:51], v[156:159]\n\t"
    "s_waitcnt lgkmcnt(6)\n\t"
    "v_mfma_f32_16x16x32_bf16 v[160:163], a[216:219], v[52:55], v[160:163]\n\t"
    "s_waitcnt lgkmcnt(5)\n\t"
    "v_mfma_f32_16x16x32_bf16 v[160:163], a[228:231], v[56:59], v[160:163]\n\t"
    "s_waitcnt lgkmcnt(4)\n\t"
    "v_mfma_f32_16x16x32_bf16 v[160:163], a[240:243], v[60:63], v[160:163]\n\t"
    "v_mfma_f32_16x16x32_bf16 v[164:167], a[220:223], v[52:55], v[164:167]\n\t"
    "ds_read_b128 v[44:47], v12 offset:6144\n\t"
    "v_mfma_f32_16x16x32_bf16 v[164:167], a[232:235], v[56:59], v[164:167]\n\t"
    "ds_read_b128 v[48:51], v12 offset:6208\n\t"
    "v_mfma_f32_16x16x32_bf16 v[164:167], a[244:247], v[60:63], v[164:167]\n\t"
    "s_waitcnt lgkmcnt(5)\n\t"
    "buffer_store_dwordx4 v[28:31], v23, s[28:31], s96 offen nt\n\t"
    "v_mfma_f32_16x16x32_bf16 v[168:171], a[224:227], v[52:55], v[168:171]\n\t"
    "ds_read_b128 v[52:55], v12 offset:6272\n\t"
    "v_mfma_f32_16x16x32_bf16 v[168:171], a[236:239], v[56:59], v[168:171]\n\t"
    "s_add_u32 s77, s49, 3072\n\t"
    "v_mfma_f32_16x16x32_bf16 v[168:171], a[248:251], v[60:63], v[168:171]\n\t"
    "v_cvt_pk_bf16_f32 v176, v156, v157\n\t"
    "s_waitcnt lgkmcnt(5)\n\t"
    "v_mfma_f32_16x16x32_bf16 v[160:163], a[252:255], v[32:35], v[160:163]\n\t"
    "v_cvt_pk_bf16_f32 v177, v158, v159\n\t"
    "s_waitcnt lgkmcnt(4)\n\t"
    "v_mfma_f32_16x16x32_bf16 v[160:163], v[196:199], v[36:39], v[160:163]\n\t"
    "ds_write_b64 v25, v[176:177] offset:0\n\t"
    "s_waitcnt lgkmcnt(4)\n\t"
    "v_mfma_f32_16x16x32_bf16 v[160:163], v[208:211], v[40:43], v[160:163]\n\t"
    "v_cvt_pk_bf16_f32 v172, v148, v149\n\t"
    "v_mfma_f32_16x16x32_bf16 v[164:167], v[188:191], v[32:35], v[164:167]\n\t"
    "v_cvt_pk_bf16_f32 v173, v150, v151\n\t"
    "v_mfma_f32_16x16x32_bf16 v[164:167], v[200:203], v[36:39], v[164:167]\n\t"
    "v_cvt_pk_bf16_f32 v174, v152, v153\n\t"
    "v_mfma_f32_16x16x32_bf16 v[164:167], v[212:215], v[40:43], v[164:167]\n\t"
    "v_cvt_pk_bf16_f32 v175, v154, v155\n\t"
    "v_mfma_f32_16x16x32_bf16 v[168:171], v[192:195], v[32:35], v[168:171]\n\t"
    "buffer_store_dwordx4 v[172:175], v22, s[28:31], s77 offen nt\n\t"
    "v_mfma_f32_16x16x32_bf16 v[168:171], v[204:207], v[36:39], v[168:171]\n\t"
    "v_mfma_f32_16x16x32_bf16 v[168:171], v[216:219], v[40:43], v[168:171]\n\t"
    "s_waitcnt lgkmcnt(3)\n\t"
    "v_mfma_f32_16x16x32_bf16 v[160:163], v[220:223], v[44:47], v[160:163]\n\t"
    "s_waitcnt lgkmcnt(2)\n\t"
    "v_mfma_f32_16x16x32_bf16 v[160:163], v[232:235], v[48:51], v[160:163]\n\t"
    "s_waitcnt lgkmcnt(1)\n\t"
    "v_mfma_f32_16x16x32_bf16 v[160:163], v[244:247], v[52:55], v[160:163]\n\t"
    "v_mfma_f32_16x16x32_bf16 v[164:167], v[224:227], v[44:47], v[164:167]\n\t"
    "v_mfma_f32_16x16x32_bf16 v[164:167], v[236:239], v[48:51], v[164:167]\n\t"
    "v_mfma_f32_16x16x32_bf16 v[164:167], v[248:251], v[52:55], v[164:167]\n\t"
    "v_mfma_f32_16x16x32_bf16 v[168:171], v[228:231], v[44:47], v[168:171]\n\t"
    "v_mfma_f32_16x16x32_bf16 v[168:171], v[240:243], v[48:51], v[168:171]\n\t"
    "v_mfma_f32_16x16x32_bf16 v[168:171], v[252:255], v[52:55], v[168:171]\n\t"
    "s_nop 7\n\ts_nop 1\n\t"
    "s_add_u32 s78, s49, 6144\n\t"
    "v_cvt_pk_bf16_f32 v182, v168, v169\n\t"
    "v_cvt_pk_bf16_f32 v183, v170, v171\n\t"
    "ds_write_b64 v25, v[182:183] offset:1280\n\t"
    "v_cvt_pk_bf16_f32 v178, v160, v161\n\t"
    "v_cvt_pk_bf16_f32 v179, v162, v163\n\t"
    "v_cvt_pk_bf16_f32 v180, v164, v165\n\t"
    "v_cvt_pk_bf16_f32 v181, v166, v167\n\t"
    "buffer_store_dwordx4 v[178:181], v22, s[28:31], s78 offen nt\n\t"
    "s_waitcnt lgkmcnt(0)\n\t"
    "s_barrier\n\t"
    "ds_read_b128 v[28:31], v26 offset:0\n\t"
    "s_add_u32 s96, s49, 3072\n\t"
    "s_add_u32 s97, s49, 6144\n\t"
    "s_cmp_eq_u32 s98, 0\n\t"
    "s_cselect_b32 s96, s96, s97\n\t"
    "s_waitcnt lgkmcnt(0)\n\t"
    "buffer_store_dwordx4 v[28:31], v23, s[28:31], s96 offen nt\n\t"
    "L_end_%=:\n\t"
    :: "s"(__builtin_amdgcn_kernarg_segment_ptr()), "s"(blockIdx.x), "v"(threadIdx.x), "s"(ldsb)
    : "v1", "v2", "v3", "v4", "v5", "v6", "v7", "v8", "v9", "v10", "v11", "v12", "v13", "v14", "v15", "v16", "v17", "v18", "v19", "v20", "v21", "v22", "v23", "v24", "v25", "v26", "v27", "v28", "v29", "v30", "v31", "v32", "v33", "v34", "v35", "v36", "v37", "v38", "v39", "v40", "v41", "v42", "v43", "v44", "v45", "v46", "v47", "v48", "v49", "v50", "v51", "v52", "v53", "v54", "v55", "v56", "v57", "v58", "v59", "v60", "v61", "v62", "v63", "v64", "v65", "v66", "v67", "v68", "v69", "v70", "v71", "v72", "v73", "v74", "v75", "v76", "v77", "v78", "v79", "v80", "v81", "v82", "v83", "v84", "v85", "v86", "v87", "v88", "v89", "v90", "v91", "v92", "v93", "v94", "v95", "v96", "v97", "v98", "v99", "v100", "v101", "v102", "v103", "v104", "v105", "v106", "v107", "v108", "v109", "v110", "v111", "v112", "v113", "v114", "v115", "v116", "v117", "v118", "v119", "v120", "v121", "v122", "v123", "v124", "v125", "v126", "v127", "v128", "v129", "v130", "v131", "v132", "v133", "v134", "v135", "v136", "v137", "v138", "v139", "v140", "v141", "v142", "v143", "v144", "v145", "v146", "v147", "v148", "v149", "v150", "v151", "v152", "v153", "v154", "v155", "v156", "v157", "v158", "v159", "v160", "v161", "v162", "v163", "v164", "v165", "v166", "v167", "v168", "v169", "v170", "v171", "v172", "v173", "v174", "v175", "v176", "v177", "v178", "v179", "v180", "v181", "v182", "v183", "v184", "v185", "v186", "v187", "v188", "v189", "v190", "v191", "v192", "v193", "v194", "v195", "v196", "v197", "v198", "v199", "v200", "v201", "v202", "v203", "v204", "v205", "v206", "v207", "v208", "v209", "v210", "v211", "v212", "v213", "v214", "v215", "v216", "v217", "v218", "v219", "v220", "v221", "v222", "v223", "v224", "v225", "v226", "v227", "v228", "v229", "v230", "v231", "v232", "v233", "v234", "v235", "v236", "v237", "v238", "v239", "v240", "v241", "v242", "v243", "v244", "v245", "v246", "v247", "v248", "v249", "v250", "v251", "v252", "v253", "v254", "v255", "a0", "a1", "a2", "a3", "a4", "a5", "a6", "a7", "a8", "a9", "a10", "a11", "a12", "a13", "a14", "a15", "a16", "a17", "a18", "a19", "a20", "a21", "a22", "a23", "a24", "a25", "a26", "a27", "a28", "a29", "a30", "a31", "a32", "a33", "a34", "a35", "a36", "a37", "a38", "a39", "a40", "a41", "a42", "a43", "a44", "a45", "a46", "a47", "a48", "a49", "a50", "a51", "a52", "a53", "a54", "a55", "a56", "a57", "a58", "a59", "a60", "a61", "a62", "a63", "a64", "a65", "a66", "a67", "a68", "a69", "a70", "a71", "a72", "a73", "a74", "a75", "a76", "a77", "a78", "a79", "a80", "a81", "a82", "a83", "a84", "a85", "a86", "a87", "a88", "a89", "a90", "a91", "a92", "a93", "a94", "a95", "a96", "a97", "a98", "a99", "a100", "a101", "a102", "a103", "a104", "a105", "a106", "a107", "a108", "a109", "a110", "a111", "a112", "a113", "a114", "a115", "a116", "a117", "a118", "a119", "a120", "a121", "a122", "a123", "a124", "a125", "a126", "a127", "a128", "a129", "a130", "a131", "a132", "a133", "a134", "a135", "a136", "a137", "a138", "a139", "a140", "a141", "a142", "a143", "a144", "a145", "a146", "a147", "a148", "a149", "a150", "a151", "a152", "a153", "a154", "a155", "a156", "a157", "a158", "a159", "a160", "a161", "a162", "a163", "a164", "a165", "a166", "a167", "a168", "a169", "a170", "a171", "a172", "a173", "a174", "a175", "a176", "a177", "a178", "a179", "a180", "a181", "a182", "a183", "a184", "a185", "a186", "a187", "a188", "a189", "a190", "a191", "a192", "a193", "a194", "a195", "a196", "a197", "a198", "a199", "a200", "a201", "a202", "a203", "a204", "a205", "a206", "a207", "a208", "a209", "a210", "a211", "a212", "a213", "a214", "a215", "a216", "a217", "a218", "a219", "a220", "a221", "a222", "a223", "a224", "a225", "a226", "a227", "a228", "a229", "a230", "a231", "a232", "a233", "a234", "a235", "a236", "a237", "a238", "a239", "a240", "a241", "a242", "a243", "a244", "a245", "a246", "a247", "a248", "a249", "a250", "a251", "a252", "a253", "a254", "a255", "s8", "s9", "s10", "s11", "s12", "s13", "s14", "s15", "s16", "s17", "s18", "s19", "s20", "s21", "s22", "s23", "s24", "s25", "s26", "s27", "s28", "s29", "s30", "s31", "s32", "s33", "s34", "s35", "s36", "s37", "s38", "s39", "s40", "s41", "s42", "s43", "s44", "s45", "s46", "s47", "s48", "s49", "s50", "s51", "s52", "s53", "s54", "s55", "s56", "s57", "s58", "s59", "s60", "s61", "s62", "s63", "s64", "s65", "s66", "s67", "s68", "s69", "s70", "s71", "s72", "s73", "s74", "s75", "s76", "s77", "s78", "s79", "s80", "s81", "s82", "s83", "s84", "s85", "s86", "s87", "s88", "s89", "s90", "s91", "s92", "s93", "s94", "s95", "s96", "s97", "s98", "s99", "vcc", "scc", "m0", "memory");
}

extern "C" void kernel_launch(void* const* d_in, const int* in_sizes, int n_in,
                              void* d_out, int out_size, void* d_ws, size_t ws_size,
                              hipStream_t stream) {
  const __hip_bfloat16* x = (const __hip_bfloat16*)d_in[0];
  const __hip_bfloat16* w = (const __hip_bfloat16*)d_in[1];
  __hip_bfloat16* out = (__hip_bfloat16*)d_out;
  conv_kernel<<<256, 256, 0, stream>>>(x, w, out);
}
```
